```python
import torch

def _softmax(x, axis=-1):
    return torch.softmax(x, dim=axis)
def _gelu(x):
    return torch.nn.functional.gelu(x, approximate="tanh")
def _silu(x):
    return torch.nn.functional.silu(x)
def _sigmoid(x):
    return torch.sigmoid(x)
def _layernorm(x, axis=-1, eps=1e-5):
    m = x.mean(dim=axis, keepdim=True)
    v = x.var(dim=axis, keepdim=True, unbiased=False)
    return (x - m) / torch.sqrt(v + eps)
def _scatter_add(tgt, idx, src, axis=0):
    return tgt.clone().index_add_(axis, idx, src)
def _segment_sum(src, seg, n):
    out = torch.zeros((n,) + src.shape[1:], dtype=src.dtype)
    return out.index_add_(0, seg, src)
def _pad(x, pw):
    flat = tuple(v for pair in reversed(pw) for v in pair)
    return torch.nn.functional.pad(x, flat)

kernel_name = 'sequential_preLN__spectral_gfnet__dense_activation_squared_relu__prompt'

def setup_inputs():

    _w = (lambda a: (lambda t: t.to(torch.bfloat16) if t.is_floating_point() else t)(torch.as_tensor(a)))
    def _A_setup():
        B, S, D = 1, 2048, 640
        Df = D // 2 + 1
        return {
            "x": _w((lambda *s: torch.randn(*s, dtype=torch.bfloat16) * 0.05)(B, S, D)),
            "w_r": _w((lambda *s: torch.randn(*s, dtype=torch.bfloat16) * 0.4)(S, Df) * 0.02),
            "w_i": _w((lambda *s: torch.randn(*s, dtype=torch.bfloat16) * 0.4)(S, Df) * 0.02),
        }
    _A_in = _A_setup()

    def _B_setup():
        B, S, D, F = 1, 2048, 640, 1536
        return {
            "x": _w((lambda *s: torch.randn(*s, dtype=torch.bfloat16) * 0.05)(B, S, D)),
            "w1": _w((lambda *s: torch.randn(*s, dtype=torch.bfloat16) * 0.05)(D, F)),
            "b1": _w((lambda *s: torch.randn(*s, dtype=torch.bfloat16) * 0.2)(F)),
            "w2": _w((lambda *s: torch.randn(*s, dtype=torch.bfloat16) * 0.05)(F, D)),
            "b2": _w((lambda *s: torch.randn(*s, dtype=torch.bfloat16) * 1.6)(D)),
        }
    _B_in = _B_setup()

    return {
        "A_x": _A_in["x"],
        "A_w_r": _A_in["w_r"],
        "A_w_i": _A_in["w_i"],
        "B_x": _B_in["x"],
        "B_w1": _B_in["w1"],
        "B_b1": _B_in["b1"],
        "B_w2": _B_in["w2"],
        "B_b2": _B_in["b2"],
    }

def reference(A_x, A_w_r, A_w_i, B_x, B_w1, B_b1, B_w2, B_b2):
    def _A_ref(x, w_r, w_i):
        B, S, D = x.shape
        X = (lambda x, axes=(-2,-1): torch.fft.rfft2(x.float(), dim=axes))(x, axes=(-2, -1))
        W = w_r + 1j * w_i
        X = X * W
        y = (lambda x, s, axes=(-2,-1): torch.fft.irfft2(x, s=s, dim=axes).to(torch.bfloat16))(X, (S, D), axes=(-2, -1))
        return y

    def _B_ref(x, w1, b1, w2, b2):
        h = torch.einsum("bsd,df->bsf", x, w1) + b1
        zero = torch.zeros_like(h)
        r = torch.where(h > zero, h, zero)
        h = r * r
        out = torch.einsum("bsf,fd->bsd", h, w2) + b2
        return out

    x = A_x
    h = x + _A_ref(_layernorm(x, axis=-1), A_w_r, A_w_i)
    h2 = h + _B_ref(_layernorm(h, axis=-1), B_w1, B_b1, B_w2, B_b2)
    return h2
```

```cpp
#include <hip/hip_runtime.h>
#include <hip/hip_bf16.h>
#include <stdint.h>

typedef __attribute__((ext_vector_type(8))) short bf16x8;
typedef __attribute__((ext_vector_type(4))) float f32x4;
typedef __attribute__((ext_vector_type(2))) float f2;
typedef __attribute__((ext_vector_type(4))) unsigned int u32x4;
typedef __attribute__((ext_vector_type(2))) unsigned int u32x2;

#define DEV __device__ __forceinline__

DEV float bf_lo(uint32_t v) { return __uint_as_float(v << 16); }
DEV float bf_hi(uint32_t v) { return __uint_as_float(v & 0xffff0000u); }
DEV float bf16r(float f) { __bf16 b = (__bf16)f; return (float)b; }
DEV uint32_t pack_bf16(float lo, float hi) {
  __bf16 a = (__bf16)lo, b = (__bf16)hi;
  return (uint32_t)__builtin_bit_cast(unsigned short, a) | ((uint32_t)__builtin_bit_cast(unsigned short, b) << 16);
}

DEV f2 cmul(f2 a, f2 b) { return f2{a.x * b.x - a.y * b.y, a.x * b.y + a.y * b.x}; }
DEV f2 cmulc(f2 a, f2 b) { return f2{a.x * b.x + a.y * b.y, a.y * b.x - a.x * b.y}; }
template <int DIR> DEV f2 ctw(f2 a, f2 w) { return DIR < 0 ? cmulc(a, w) : cmul(a, w); }
template <int DIR> DEV f2 mul_i(f2 a) { return DIR < 0 ? f2{a.y, -a.x} : f2{-a.y, a.x}; }
DEV f2 cis(float rev) { return f2{__builtin_amdgcn_cosf(rev), __builtin_amdgcn_sinf(rev)}; }

template <int DIR> DEV void fft4(f2& a0, f2& a1, f2& a2, f2& a3) {
  f2 b0 = a0 + a2, b1 = a0 - a2, b2 = a1 + a3, b3 = mul_i<DIR>(a1 - a3);
  a0 = b0 + b2; a2 = b0 - b2; a1 = b1 + b3; a3 = b1 - b3;
}
template <int DIR> DEV void fft8(f2* a) {
  const float R = 0.70710678118654752f;
  f2 t0 = a[0] + a[4], t4 = a[0] - a[4];
  f2 t1 = a[1] + a[5], t5 = a[1] - a[5];
  f2 t2 = a[2] + a[6], t6 = a[2] - a[6];
  f2 t3 = a[3] + a[7], t7 = a[3] - a[7];
  if (DIR < 0) {
    t5 = f2{(t5.x + t5.y) * R, (t5.y - t5.x) * R};
    t7 = f2{(t7.y - t7.x) * R, -(t7.x + t7.y) * R};
  } else {
    t5 = f2{(t5.x - t5.y) * R, (t5.x + t5.y) * R};
    t7 = f2{-(t7.x + t7.y) * R, (t7.x - t7.y) * R};
  }
  t6 = mul_i<DIR>(t6);
  fft4<DIR>(t0, t1, t2, t3);
  fft4<DIR>(t4, t5, t6, t7);
  a[0] = t0; a[2] = t1; a[4] = t2; a[6] = t3;
  a[1] = t4; a[3] = t5; a[5] = t6; a[7] = t7;
}
template <int DIR> DEV void fft5(f2* a) {
  const float c1 = 0.30901699437494742f, c2 = -0.80901699437494742f;
  const float s1 = 0.95105651629515357f, s2 = 0.58778525229247313f;
  f2 t1 = a[1] + a[4], t2 = a[2] + a[3], t3 = a[1] - a[4], t4 = a[2] - a[3];
  f2 a1 = a[0] + c1 * t1 + c2 * t2, a2 = a[0] + c2 * t1 + c1 * t2;
  f2 b1 = mul_i<DIR>(s1 * t3 + s2 * t4), b2 = mul_i<DIR>(s2 * t3 - s1 * t4);
  a[0] = a[0] + t1 + t2;
  a[1] = a1 + b1; a[4] = a1 - b1; a[2] = a2 + b2; a[3] = a2 - b2;
}

template <int CTRL> DEV float dpp_f(float v) {
  return __int_as_float(__builtin_amdgcn_update_dpp(0, __float_as_int(v), CTRL, 0xf, 0xf, true));
}
DEV float wave_sum(float v) {
  v += dpp_f<0xB1>(v);
  v += dpp_f<0x4E>(v);
  v += dpp_f<0x141>(v);
  v += dpp_f<0x140>(v);
  int iv = __float_as_int(v);
  float a = __int_as_float(__builtin_amdgcn_readlane(iv, 0));
  float b = __int_as_float(__builtin_amdgcn_readlane(iv, 16));
  float c = __int_as_float(__builtin_amdgcn_readlane(iv, 32));
  float d = __int_as_float(__builtin_amdgcn_readlane(iv, 48));
  return (a + b) + (c + d);
}
#define WSYNC() __builtin_amdgcn_wave_barrier()

DEV void layernorm10(float* v) {
  float s = 0.f;
#pragma unroll
  for (int i = 0; i < 10; ++i) s += v[i];
  const float mean = wave_sum(s) * (1.0f / 640.0f);
  float q = 0.f;
#pragma unroll
  for (int i = 0; i < 10; ++i) { float d = v[i] - mean; q += d * d; }
  const float var = wave_sum(q) * (1.0f / 640.0f);
  const float mb = bf16r(mean);
  const float den = bf16r(sqrtf(bf16r(bf16r(var) + 1e-5f)));
  const float rd = 1.0f / den;
#pragma unroll
  for (int i = 0; i < 10; ++i) v[i] = bf16r(bf16r(v[i] - mb) * rd);
}

DEV int slotaddr(int k, int r) {
  if (k == 0) return r <= 4 ? r * 16 : (8 - r) * 16 + 1;
  if (k == 160) return (r == 0 || r == 4) ? r * 16 + 1 : (r >= 5 ? r * 16 : (8 - r) * 16 + 1);
  if (k < 160) return ((k >> 3) * 8 + r) * 16 + 2 * (k & 7);
  int m = 320 - k;
  return ((m >> 3) * 8 + ((8 - r) & 7)) * 16 + 2 * (m & 7) + 1;
}

constexpr int ROWB = 2880;

__global__ __launch_bounds__(512) void k1_kernel(const uint32_t* __restrict__ x, const uint16_t* __restrict__ wr,
                                                 const uint16_t* __restrict__ wi, f2* __restrict__ T,
                                                 uint32_t* __restrict__ WtA, uint32_t* __restrict__ WtB,
                                                 uint32_t* __restrict__ WnA, uint32_t* __restrict__ WnB) {
  __shared__ __attribute__((aligned(16))) char smem[8 * ROWB];
  const int tid = threadIdx.x, wave = tid >> 6, lane = tid & 63;
  if (blockIdx.x >= 256) {
    const int b = blockIdx.x - 256;
    const int r = b & 7, a = (b >> 3) & 7, kq = b >> 6;
    uint32_t* tile = (uint32_t*)smem;
    const int ncol = (kq == 3) ? 81 : 80;
    for (int e = tid; e < 32 * 81; e += 512) {
      int ridx = e / 81, kk = e - ridx * 81;
      if (kk < ncol) {
        int up = 4 * a + (ridx >> 3) + 32 * (ridx & 7);
        int u = 8 * up + r;
        int o = u * 321 + 80 * kq + kk;
        tile[e] = (uint32_t)wr[o] | ((uint32_t)wi[o] << 16);
      }
    }
    __syncthreads();
    for (int e = tid; e < 32 * ncol; e += 512) {
      int ridx = e & 31, kk = e >> 5;
      int k = 80 * kq + kk;
      uint32_t val = tile[ridx * 81 + kk];
      int ee = 4 * a + (ridx >> 3), b2 = ridx & 7;
      int up = ee + 32 * b2;
      int upp = (r == 0) ? ((256 - up) & 255) : (255 - up);
      int idxp = (upp & 31) * 8 + (upp >> 5);
      int rp = (8 - r) & 7;
      if (k < 320) {
        WtA[slotaddr(k, r) * 256 + ee * 8 + b2] = val;
        WtB[slotaddr(k == 0 ? 0 : 320 - k, rp) * 256 + idxp] = val;
      } else {
        WnA[r * 256 + ee * 8 + b2] = val;
        WnB[rp * 256 + idxp] = val;
      }
    }
    return;
  }
  const int s0 = blockIdx.x;
  const int s = s0 + 256 * wave;
  uint32_t raw[5];
#pragma unroll
  for (int q = 0; q < 5; ++q) raw[q] = x[s * 320 + 64 * q + lane];
  float v[10];
#pragma unroll
  for (int q = 0; q < 5; ++q) { v[2 * q] = bf_lo(raw[q]); v[2 * q + 1] = bf_hi(raw[q]); }
  layernorm10(v);
  char* rb = smem + wave * ROWB;
  {
    f2 z[5];
#pragma unroll
    for (int q = 0; q < 5; ++q) z[q] = f2{v[2 * q], v[2 * q + 1]};
    fft5<-1>(z);
#pragma unroll
    for (int k1 = 1; k1 < 5; ++k1) z[k1] = ctw<-1>(z[k1], cis((float)(lane * k1) * (1.0f / 320.0f)));
#pragma unroll
    for (int k1 = 0; k1 < 5; ++k1) *(f2*)(rb + k1 * 576 + lane * 8) = z[k1];
  }
  WSYNC();
  if (lane < 40) {
    const int k1 = lane >> 3, lo = lane & 7;
    f2 a[8];
#pragma unroll
    for (int l1 = 0; l1 < 8; ++l1) a[l1] = *(f2*)(rb + k1 * 576 + (8 * l1 + lo) * 8);
    fft8<-1>(a);
#pragma unroll
    for (int m1 = 1; m1 < 8; ++m1) a[m1] = ctw<-1>(a[m1], cis((float)(lo * m1) * (1.0f / 64.0f)));
    WSYNC();
#pragma unroll
    for (int m1 = 0; m1 < 8; ++m1) *(f2*)(rb + k1 * 576 + m1 * 72 + lo * 8) = a[m1];
    WSYNC();
#pragma unroll
    for (int l2 = 0; l2 < 8; ++l2) a[l2] = *(f2*)(rb + k1 * 576 + lo * 72 + l2 * 8);
    fft8<-1>(a);
    WSYNC();
#pragma unroll
    for (int m2 = 0; m2 < 8; ++m2) *(f2*)(rb + (k1 + 5 * lo + 40 * m2) * 8) = a[m2];
  }
  __syncthreads();
  if (tid < 320) {
    const int g = tid >> 4, pos = tid & 15, side = pos & 1, m = 8 * g + (pos >> 1);
    const int k = (m == 0) ? (side ? 160 : 0) : (side ? 320 - m : m);
    f2 a[8];
#pragma unroll
    for (int jj = 0; jj < 8; ++jj) {
      int row = side ? ((8 - jj) & 7) : jj;
      a[jj] = *(f2*)(smem + row * ROWB + k * 8);
    }
    fft8<-1>(a);
#pragma unroll
    for (int rr = 1; rr < 8; ++rr) {
      int r = side ? 8 - rr : rr;
      a[rr] = ctw<-1>(a[rr], cis((float)(s0 * r) * (1.0f / 2048.0f)));
    }
#pragma unroll
    for (int rr = 0; rr < 8; ++rr) {
      int ch = g * 8 + rr, p = pos;
      if (tid == 0 && rr >= 5) { ch = 8 - rr; p = 1; }
      if (tid == 1 && rr >= 1 && rr <= 3) { ch = 8 - rr; p = 0; }
      T[(ch * 256 + s0) * 16 + p] = a[rr];
    }
  }
}

constexpr int K2_AS = 1152, K2_PS = 4624;
__global__ __launch_bounds__(512) void k2_kernel(const f2* __restrict__ T, f2* __restrict__ T2,
                                                 const uint32_t* __restrict__ WtA, const uint32_t* __restrict__ WtB,
                                                 const uint32_t* __restrict__ WnA, const uint32_t* __restrict__ WnB,
                                                 const uint16_t* __restrict__ w1, const uint16_t* __restrict__ w2,
                                                 u32x4* __restrict__ W1f, u32x4* __restrict__ W2f) {
  __shared__ __attribute__((aligned(16))) char smem[8 * K2_PS];
  const int tid = threadIdx.x, wave = tid >> 6, lane = tid & 63;
  if (blockIdx.x >= 160) {
    int gid = (blockIdx.x - 160) * 512 + tid;
    u32x2 v[8];
    if (gid < 30720) {
      int fq = gid % 384, kh = gid / 384;
#pragma unroll
      for (int e = 0; e < 8; ++e) v[e] = *(const u32x2*)(w1 + (8 * kh + e) * 1536 + 4 * fq);
      int ks = kh >> 2, h = kh & 3;
#pragma unroll
      for (int ff = 0; ff < 4; ++ff) {
        int f = 4 * fq + ff, j = f / 96, fr = f - 96 * j, t = fr >> 4, rho = fr & 15;
        u32x4 o;
#pragma unroll
        for (int e = 0; e < 4; ++e) {
          uint32_t lo = v[2 * e][ff >> 1], hi = v[2 * e + 1][ff >> 1];
          o[e] = (ff & 1) ? ((lo >> 16) | (hi & 0xffff0000u)) : ((lo & 0xffffu) | (hi << 16));
        }
        W1f[((j * 20 + ks) * 6 + t) * 64 + h * 16 + rho] = o;
      }
    } else {
      gid -= 30720;
      int dq = gid % 160, kh = gid / 160;
      int ks = kh >> 2, h = kh & 3;
#pragma unroll
      for (int e = 0; e < 8; ++e) {
        int f = 32 * ks + (e < 4 ? 4 * h + e : 16 + 4 * h + e - 4);
        v[e] = *(const u32x2*)(w2 + f * 640 + 4 * dq);
      }
#pragma unroll
      for (int ff = 0; ff < 4; ++ff) {
        int d = 4 * dq + ff, jd = d / 80, dr = d - 80 * jd, t = dr >> 4, rho = dr & 15;
        u32x4 o;
#pragma unroll
        for (int e = 0; e < 4; ++e) {
          uint32_t lo = v[2 * e][ff >> 1], hi = v[2 * e + 1][ff >> 1];
          o[e] = (ff & 1) ? ((lo >> 16) | (hi & 0xffff0000u)) : ((lo & 0xffffu) | (hi << 16));
        }
        W2f[((jd * 48 + ks) * 5 + t) * 64 + h * 16 + rho] = o;
      }
    }
    return;
  }
  const int chunk = blockIdx.x, g = chunk >> 3, rr = chunk & 7;
  const int pp = tid & 7, c = tid >> 3;
  const f32x4* Tin = (const f32x4*)(T + (size_t)chunk * 4096);
  f32x4 ld[4];
#pragma unroll
  for (int i = 0; i < 4; ++i) ld[i] = Tin[i * 512 + tid];
  const int a1 = lane >> 4, xx = (lane >> 1) & 7, side = lane & 1;
  const int pos = 2 * wave + side;
  int k, r;
  bool selfp = false;
  {
    int m = 8 * g + wave;
    if (m != 0) { k = side ? 320 - m : m; r = side ? ((8 - rr) & 7) : rr; }
    else if (rr == 0 || rr == 4) { k = side ? 160 : 0; r = rr; selfp = true; }
    else { k = rr < 4 ? 0 : 160; r = side ? 8 - rr : rr; }
  }
  const int e = a1 + 4 * xx;
  u32x4 wa[2], wb[2];
  {
    const u32x4* pa = (const u32x4*)(WtA + ((size_t)(chunk * 16 + pos) * 256 + e * 8));
    const u32x4* pb = (const u32x4*)(WtB + ((size_t)(chunk * 16 + pos) * 256 + e * 8));
    wa[0] = pa[0]; wa[1] = pa[1]; wb[0] = pb[0]; wb[1] = pb[1];
  }
  u32x4 na[2] = {}, nb[2] = {};
  if (k == 0) {
    const u32x4* pa = (const u32x4*)(WnA + (r * 256 + e * 8));
    const u32x4* pb = (const u32x4*)(WnB + (r * 256 + e * 8));
    na[0] = pa[0]; na[1] = pa[1]; nb[0] = pb[0]; nb[1] = pb[1];
  }
  f2 tw1[4], tw2[8];
#pragma unroll
  for (int i = 1; i < 4; ++i) tw1[i] = cis((float)(c * i) * (1.0f / 256.0f));
#pragma unroll
  for (int i = 1; i < 8; ++i) tw2[i] = cis((float)(xx * i) * (1.0f / 64.0f));

  {
    f2 p[4], q[4];
#pragma unroll
    for (int i = 0; i < 4; ++i) { p[i] = f2{ld[i].x, ld[i].y}; q[i] = f2{ld[i].z, ld[i].w}; }
    fft4<-1>(p[0], p[1], p[2], p[3]);
    fft4<-1>(q[0], q[1], q[2], q[3]);
#pragma unroll
    for (int i = 1; i < 4; ++i) { p[i] = ctw<-1>(p[i], tw1[i]); q[i] = ctw<-1>(q[i], tw1[i]); }
#pragma unroll
    for (int i = 0; i < 4; ++i)
      *(f32x4*)(smem + pp * K2_PS + i * K2_AS + c * 16) = f32x4{p[i].x, p[i].y, q[i].x, q[i].y};
  }
  __syncthreads();
  char* wb_ = smem + wave * K2_PS;
  f2 a[8];
#pragma unroll
  for (int c1 = 0; c1 < 8; ++c1) a[c1] = *(f2*)(wb_ + a1 * K2_AS + (8 * c1 + xx) * 16 + side * 8);
  fft8<-1>(a);
#pragma unroll
  for (int b1 = 1; b1 < 8; ++b1) a[b1] = ctw<-1>(a[b1], tw2[b1]);
  WSYNC();
#pragma unroll
  for (int b1 = 0; b1 < 8; ++b1) *(f2*)(wb_ + a1 * 1152 + b1 * 144 + xx * 16 + side * 8) = a[b1];
  WSYNC();
#pragma unroll
  for (int c2 = 0; c2 < 8; ++c2) a[c2] = *(f2*)(wb_ + a1 * 1152 + xx * 144 + c2 * 16 + side * 8);
  fft8<-1>(a);
  WSYNC();
#pragma unroll
  for (int b2 = 0; b2 < 8; ++b2) {
    int up = e + 32 * b2;
    *(f2*)(wb_ + (up + (up >> 3)) * 16 + side * 8) = a[b2];
  }
  WSYNC();
  {
    const int pside = selfp ? side : (side ^ 1);
    const f2 th = cis((float)k * (1.0f / 640.0f));
    const float sc = 0.5f / (2048.0f * 320.0f);
    const float cm = (1.0f - th.y) * sc, cp = (1.0f + th.y) * sc, cc = th.x * sc;
#pragma unroll
    for (int b2 = 0; b2 < 8; ++b2) {
      int up = e + 32 * b2;
      int upp = (r == 0) ? ((256 - up) & 255) : (255 - up);
      f2 zb = *(f2*)(wb_ + (upp + (upp >> 3)) * 16 + pside * 8);
      uint32_t ua = wa[b2 >> 2][b2 & 3], ub = wb[b2 >> 2][b2 & 3];
      f2 A = f2{bf_lo(ua), bf_hi(ua)};
      f2 Bc = f2{bf_lo(ub), -bf_hi(ub)};
      if (k == 0) {
        uint32_t va = na[b2 >> 2][b2 & 3], vb = nb[b2 >> 2][b2 & 3];
        A = 0.5f * (A + Bc);
        Bc = 0.5f * (f2{bf_lo(va), bf_hi(va)} + f2{bf_lo(vb), -bf_hi(vb)});
      }
      f2 al = cm * A + cp * Bc;
      f2 dd = A - Bc;
      f2 be = f2{-cc * dd.y, cc * dd.x};
      f2 zbc = f2{zb.x, -zb.y};
      a[b2] = cmul(al, a[b2]) + cmul(be, zbc);
    }
  }
  fft8<1>(a);
  WSYNC();
#pragma unroll
  for (int c2 = 0; c2 < 8; ++c2) *(f2*)(wb_ + a1 * 1152 + xx * 144 + c2 * 16 + side * 8) = a[c2];
  WSYNC();
#pragma unroll
  for (int b1 = 0; b1 < 8; ++b1) a[b1] = *(f2*)(wb_ + a1 * 1152 + b1 * 144 + xx * 16 + side * 8);
#pragma unroll
  for (int b1 = 1; b1 < 8; ++b1) a[b1] = ctw<1>(a[b1], tw2[b1]);
  fft8<1>(a);
  WSYNC();
#pragma unroll
  for (int c1 = 0; c1 < 8; ++c1) *(f2*)(wb_ + a1 * K2_AS + (8 * c1 + xx) * 16 + side * 8) = a[c1];
  __syncthreads();
  {
    f2 p[4], q[4];
#pragma unroll
    for (int i = 0; i < 4; ++i) {
      f32x4 t = *(f32x4*)(smem + pp * K2_PS + i * K2_AS + c * 16);
      p[i] = f2{t.x, t.y}; q[i] = f2{t.z, t.w};
    }
#pragma unroll
    for (int i = 1; i < 4; ++i) { p[i] = ctw<1>(p[i], tw1[i]); q[i] = ctw<1>(q[i], tw1[i]); }
    fft4<1>(p[0], p[1], p[2], p[3]);
    fft4<1>(q[0], q[1], q[2], q[3]);
    f32x4* Tout = (f32x4*)(T2 + (size_t)chunk * 4096);
#pragma unroll
    for (int i = 0; i < 4; ++i) Tout[i * 512 + tid] = f32x4{p[i].x, p[i].y, q[i].x, q[i].y};
  }
}

__global__ __launch_bounds__(512) void k3_kernel(const uint32_t* __restrict__ x, const f2* __restrict__ T2,
                                                 uint32_t* __restrict__ hbuf, u32x4* __restrict__ L2f) {
  __shared__ __attribute__((aligned(16))) char smem[8 * ROWB];
  const int tid = threadIdx.x, wave = tid >> 6, lane = tid & 63;
  const int s0 = blockIdx.x;
  const int s = s0 + 256 * wave;
  uint32_t raw[5];
#pragma unroll
  for (int q = 0; q < 5; ++q) raw[q] = x[s * 320 + 64 * q + lane];
  if (tid < 320) {
    const int g = tid >> 4, pos = tid & 15, side = pos & 1, m = 8 * g + (pos >> 1);
    const int k = (m == 0) ? (side ? 160 : 0) : (side ? 320 - m : m);
    f2 a[8];
#pragma unroll
    for (int rr = 0; rr < 8; ++rr) {
      int ch = g * 8 + rr, p = pos;
      if (tid == 0 && rr >= 5) { ch = 8 - rr; p = 1; }
      if (tid == 1 && rr >= 1 && rr <= 3) { ch = 8 - rr; p = 0; }
      a[rr] = T2[(ch * 256 + s0) * 16 + p];
    }
#pragma unroll
    for (int rr = 1; rr < 8; ++rr) {
      int r = side ? 8 - rr : rr;
      a[rr] = ctw<1>(a[rr], cis((float)(s0 * r) * (1.0f / 2048.0f)));
    }
    fft8<1>(a);
#pragma unroll
    for (int jj = 0; jj < 8; ++jj) {
      int row = side ? ((8 - jj) & 7) : jj;
      *(f2*)(smem + row * ROWB + k * 8) = a[jj];
    }
  }
  __syncthreads();
  char* rb = smem + wave * ROWB;
  if (lane < 40) {
    const int k1 = lane >> 3, lo = lane & 7;
    f2 a[8];
#pragma unroll
    for (int m2 = 0; m2 < 8; ++m2) a[m2] = *(f2*)(rb + (k1 + 5 * lo + 40 * m2) * 8);
    fft8<1>(a);
    WSYNC();
#pragma unroll
    for (int l2 = 0; l2 < 8; ++l2) *(f2*)(rb + k1 * 576 + lo * 72 + l2 * 8) = a[l2];
    WSYNC();
#pragma unroll
    for (int m1 = 0; m1 < 8; ++m1) a[m1] = *(f2*)(rb + k1 * 576 + m1 * 72 + lo * 8);
#pragma unroll
    for (int m1 = 1; m1 < 8; ++m1) a[m1] = ctw<1>(a[m1], cis((float)(lo * m1) * (1.0f / 64.0f)));
    fft8<1>(a);
    WSYNC();
#pragma unroll
    for (int l1 = 0; l1 < 8; ++l1) *(f2*)(rb + k1 * 576 + (8 * l1 + lo) * 8) = a[l1];
  }
  WSYNC();
  float v[10];
  {
    f2 z[5];
#pragma unroll
    for (int k1 = 0; k1 < 5; ++k1) z[k1] = *(f2*)(rb + k1 * 576 + lane * 8);
#pragma unroll
    for (int k1 = 1; k1 < 5; ++k1) z[k1] = ctw<1>(z[k1], cis((float)(lane * k1) * (1.0f / 320.0f)));
    fft5<1>(z);
#pragma unroll
    for (int q = 0; q < 5; ++q) {
      v[2 * q] = bf16r(bf_lo(raw[q]) + bf16r(z[q].x));
      v[2 * q + 1] = bf16r(bf_hi(raw[q]) + bf16r(z[q].y));
      hbuf[s * 320 + 64 * q + lane] = pack_bf16(v[2 * q], v[2 * q + 1]);
    }
  }
  layernorm10(v);
  WSYNC();
#pragma unroll
  for (int q = 0; q < 5; ++q) *(uint32_t*)(rb + (64 * q + lane) * 4) = pack_bf16(v[2 * q], v[2 * q + 1]);
  __syncthreads();
  const int mt = s0 >> 1;
  for (int p = tid; p < 640; p += 512) {
    int j = p & 7, kh = p >> 3;
    u32x4 val = *(u32x4*)(smem + j * ROWB + kh * 16);
    L2f[(mt * 20 + (kh >> 2)) * 64 + (kh & 3) * 16 + 8 * (s0 & 1) + j] = val;
  }
}

__global__ __launch_bounds__(256) void k4_kernel(const u32x4* __restrict__ L2f, const u32x4* __restrict__ W1f,
                                                 const uint16_t* __restrict__ b1, u32x4* __restrict__ Hf) {
  __shared__ u32x4 lds[7680];
  const int tid = threadIdx.x, wave = tid >> 6, lane = tid & 63;
  const int b = blockIdx.x, xcd = b & 7, idx = b >> 3;
  const int i = (xcd >> 1) * 4 + (idx & 3), j = (xcd & 1) * 8 + (idx >> 2);
  const int mt0 = 8 * i + 2 * wave;
  u32x4 wreg[30];
  u32x4 bfr[2][20];
  const u32x4* wsrc = W1f + (size_t)j * 7680;
#pragma unroll
  for (int p = 0; p < 5; ++p) {
#pragma unroll
    for (int n = 0; n < 6; ++n) wreg[6 * p + n] = wsrc[(6 * p + n) * 256 + tid];
#pragma unroll
    for (int kk = 0; kk < 4; ++kk) {
      bfr[0][4 * p + kk] = L2f[((mt0 + 0) * 20 + 4 * p + kk) * 64 + lane];
      bfr[1][4 * p + kk] = L2f[((mt0 + 1) * 20 + 4 * p + kk) * 64 + lane];
    }
  }
  f32x4 acc[6][2];
#pragma unroll
  for (int t = 0; t < 6; ++t) { acc[t][0] = f32x4{0, 0, 0, 0}; acc[t][1] = f32x4{0, 0, 0, 0}; }
#pragma unroll
  for (int n = 0; n < 6; ++n) lds[n * 256 + tid] = wreg[n];
  __syncthreads();
#pragma unroll
  for (int p = 0; p < 5; ++p) {
    if (p < 4) {
#pragma unroll
      for (int n = 0; n < 6; ++n) lds[(6 * (p + 1) + n) * 256 + tid] = wreg[6 * (p + 1) + n];
    }
#pragma unroll
    for (int kk = 0; kk < 4; ++kk) {
      const int ks = 4 * p + kk;
#pragma unroll
      for (int t = 0; t < 6; ++t) {
        bf16x8 af = __builtin_bit_cast(bf16x8, lds[(ks * 6 + t) * 64 + lane]);
        acc[t][0] = __builtin_amdgcn_mfma_f32_16x16x32_bf16(af, __builtin_bit_cast(bf16x8, bfr[0][ks]), acc[t][0], 0, 0, 0);
        acc[t][1] = __builtin_amdgcn_mfma_f32_16x16x32_bf16(af, __builtin_bit_cast(bf16x8, bfr[1][ks]), acc[t][1], 0, 0, 0);
      }
    }
    if (p < 4) __syncthreads();
  }
  const int h = lane >> 4;
#pragma unroll
  for (int tp = 0; tp < 3; ++tp) {
    float bb[8];
#pragma unroll
    for (int half = 0; half < 2; ++half) {
      u32x2 bv = *(const u32x2*)(b1 + 96 * j + 16 * (2 * tp + half) + 4 * h);
      bb[4 * half + 0] = bf_lo(bv.x); bb[4 * half + 1] = bf_hi(bv.x);
      bb[4 * half + 2] = bf_lo(bv.y); bb[4 * half + 3] = bf_hi(bv.y);
    }
#pragma unroll
    for (int mi = 0; mi < 2; ++mi) {
      float o[8];
#pragma unroll
      for (int e = 0; e < 8; ++e) {
        float a = acc[2 * tp + (e >> 2)][mi][e & 3];
        float hh = bf16r(bf16r(a) + bb[e]);
        hh = hh > 0.f ? hh : 0.f;
        o[e] = hh * hh;
      }
      u32x4 pk;
#pragma unroll
      for (int e = 0; e < 4; ++e) pk[e] = pack_bf16(o[2 * e], o[2 * e + 1]);
      Hf[((mt0 + mi) * 48 + 3 * j + tp) * 64 + lane] = pk;
    }
  }
}

__global__ __launch_bounds__(256) void k5_kernel(const u32x4* __restrict__ Hf, const u32x4* __restrict__ W2f,
                                                 const uint16_t* __restrict__ b2, const uint16_t* __restrict__ hbuf,
                                                 uint16_t* __restrict__ out) {
  __shared__ f32x4 red[4 * 4 * 5 * 64];
  const int tid = threadIdx.x, wave = tid >> 6, lane = tid & 63;
  const int b = blockIdx.x, xcd = b & 7, idx = b >> 3;
  const int i = (xcd >> 1) * 8 + (idx & 7), jd = (xcd & 1) * 4 + (idx >> 3);
  f32x4 acc[5][4];
#pragma unroll
  for (int t = 0; t < 5; ++t)
#pragma unroll
    for (int mi = 0; mi < 4; ++mi) acc[t][mi] = f32x4{0, 0, 0, 0};
  const u32x4* hsrc = Hf + (size_t)(4 * i) * 48 * 64 + lane;
  const u32x4* wsrc = W2f + (size_t)jd * 48 * 5 * 64 + lane;
#pragma unroll
  for (int half = 0; half < 2; ++half) {
    u32x4 hf[6][4], wf[6][5];
#pragma unroll
    for (int kk = 0; kk < 6; ++kk) {
      const int ks = 12 * wave + 6 * half + kk;
#pragma unroll
      for (int mi = 0; mi < 4; ++mi) hf[kk][mi] = hsrc[(mi * 48 + ks) * 64];
#pragma unroll
      for (int t = 0; t < 5; ++t) wf[kk][t] = wsrc[(ks * 5 + t) * 64];
    }
#pragma unroll
    for (int kk = 0; kk < 6; ++kk)
#pragma unroll
      for (int t = 0; t < 5; ++t)
#pragma unroll
        for (int mi = 0; mi < 4; ++mi)
          acc[t][mi] = __builtin_amdgcn_mfma_f32_16x16x32_bf16(__builtin_bit_cast(bf16x8, wf[kk][t]),
                                                               __builtin_bit_cast(bf16x8, hf[kk][mi]), acc[t][mi], 0, 0, 0);
  }
#pragma unroll
  for (int mi = 0; mi < 4; ++mi)
#pragma unroll
    for (int t = 0; t < 5; ++t) red[((wave * 4 + mi) * 5 + t) * 64 + lane] = acc[t][mi];
  __syncthreads();
  const int m = lane & 15, h = lane >> 4;
  const int mp = 16 * (4 * i + wave) + m;
  const int s = (mp >> 3) + 256 * (mp & 7);
#pragma unroll
  for (int t = 0; t < 5; ++t) {
    f32x4 sum = red[((0 * 4 + wave) * 5 + t) * 64 + lane];
#pragma unroll
    for (int w = 1; w < 4; ++w) sum += red[((w * 4 + wave) * 5 + t) * 64 + lane];
    const int d = 80 * jd + 16 * t + 4 * h;
    u32x2 bv = *(const u32x2*)(b2 + d);
    u32x2 hv = *(const u32x2*)(hbuf + s * 640 + d);
    float o0 = bf16r(bf16r(sum.x) + bf_lo(bv.x)) + bf_lo(hv.x);
    float o1 = bf16r(bf16r(sum.y) + bf_hi(bv.x)) + bf_hi(hv.x);
    float o2 = bf16r(bf16r(sum.z) + bf_lo(bv.y)) + bf_lo(hv.y);
    float o3 = bf16r(bf16r(sum.w) + bf_hi(bv.y)) + bf_hi(hv.y);
    *(u32x2*)(out + s * 640 + d) = u32x2{pack_bf16(o0, o1), pack_bf16(o2, o3)};
  }
}

extern "C" void kernel_launch(void* const* d_in, const int* in_sizes, int n_in, void* d_out, int out_size, void* d_ws,
                              size_t ws_size, hipStream_t stream) {
  const uint32_t* x = (const uint32_t*)d_in[0];
  const uint16_t* wr = (const uint16_t*)d_in[1];
  const uint16_t* wi = (const uint16_t*)d_in[2];
  const uint16_t* w1 = (const uint16_t*)d_in[4];
  const uint16_t* b1 = (const uint16_t*)d_in[5];
  const uint16_t* w2 = (const uint16_t*)d_in[6];
  const uint16_t* b2 = (const uint16_t*)d_in[7];
  char* ws = (char*)d_ws;
  size_t off = 0;
  auto carve = [&](size_t n) { char* p = ws + off; off += (n + 4095) & ~(size_t)4095; return p; };
  f2* T = (f2*)carve(5242880);
  f2* T2 = (f2*)carve(5242880);
  uint32_t* WtA = (uint32_t*)carve(2621440);
  uint32_t* WtB = (uint32_t*)carve(2621440);
  uint32_t* WnA = (uint32_t*)carve(8192);
  uint32_t* WnB = (uint32_t*)carve(8192);
  u32x4* W1f = (u32x4*)carve(1966080);
  u32x4* W2f = (u32x4*)carve(1966080);
  u32x4* L2f = (u32x4*)carve(2621440);
  uint32_t* hbuf = (uint32_t*)carve(2621440);
  u32x4* Hf = (u32x4*)carve(6291456);
  k1_kernel<<<512, 512, 0, stream>>>(x, wr, wi, T, WtA, WtB, WnA, WnB);
  k2_kernel<<<280, 512, 0, stream>>>(T, T2, WtA, WtB, WnA, WnB, w1, w2, W1f, W2f);
  k3_kernel<<<256, 512, 0, stream>>>(x, T2, hbuf, L2f);
  k4_kernel<<<256, 256, 0, stream>>>(L2f, W1f, b1, Hf);
  k5_kernel<<<256, 256, 0, stream>>>(Hf, W2f, b2, (const uint16_t*)hbuf, (uint16_t*)d_out);
}
```

```cpp
#include <hip/hip_runtime.h>
#include <hip/hip_bf16.h>
#include <stdint.h>

typedef __attribute__((ext_vector_type(8))) short bf16x8;
typedef __attribute__((ext_vector_type(4))) float f32x4;
typedef __attribute__((ext_vector_type(2))) float f2;
typedef __attribute__((ext_vector_type(4))) unsigned int u32x4;
typedef __attribute__((ext_vector_type(2))) unsigned int u32x2;
typedef u32x2 u32x2_a2 __attribute__((aligned(2)));
typedef u32x4 u32x4_a2 __attribute__((aligned(2)));

#define DEV __device__ __forceinline__
constexpr int K2_LINES = 44, K3_LINES = 140;
#define K3SYM "_Z11k345_kernelPKjPKDv2_fPjPDv4_jPKS5_PKtS6_S8_SA_PtS4_"
#define K2SYM "_Z9k2_kernelPKDv2_fPS_PKDv4_jS5_PKtS7_PS3_S8_Pj"

constexpr int TL1 = 0;
constexpr int TL2 = 128;
constexpr int TW640 = 1152;
constexpr int TW2K = 1320;
__device__ __attribute__((aligned(16))) const float g_twf[3368 * 2] = {
1.f,0.f,1.f,0.f,0.99980724f,0.0196336925f,0.995184727f,0.0980171403f,0.999229036f,0.0392598158f,0.98078528f,0.195090322f,
0.99826561f,0.0588708037f,0.956940336f,0.290284677f,0.996917334f,0.0784590957f,0.923879533f,0.382683432f,0.995184727f,0.0980171403f,0.881921264f,0.471396737f,
0.993068457f,0.117537397f,0.831469612f,0.555570233f,0.99056934f,0.137012342f,0.773010453f,0.634393284f,0.987688341f,0.156434465f,1.f,0.f,
0.984426568f,0.17579628f,0.995184727f,0.0980171403f,0.98078528f,0.195090322f,0.98078528f,0.195090322f,0.976765881f,0.214309153f,0.956940336f,0.290284677f,
0.97236992f,0.233445364f,0.923879533f,0.382683432f,0.967599092f,0.252491577f,0.881921264f,0.471396737f,0.962455236f,0.27144045f,0.831469612f,0.555570233f,
0.956940336f,0.290284677f,0.773010453f,0.634393284f,0.951056516f,0.309016994f,1.f,0.f,0.944806046f,0.32763018f,0.995184727f,0.0980171403f,
0.938191336f,0.346117057f,0.98078528f,0.195090322f,0.931214935f,0.3644705f,0.956940336f,0.290284677f,0.923879533f,0.382683432f,0.923879533f,0.382683432f,
0.916187957f,0.400748833f,0.881921264f,0.471396737f,0.908143174f,0.418659738f,0.831469612f,0.555570233f,0.899748284f,0.436409241f,0.773010453f,0.634393284f,
0.891006524f,0.4539905f,1.f,0.f,0.881921264f,0.471396737f,0.995184727f,0.0980171403f,0.872496007f,0.488621241f,0.98078528f,0.195090322f,
0.862734386f,0.505657373f,0.956940336f,0.290284677f,0.852640164f,0.522498565f,0.923879533f,0.382683432f,0.842217234f,0.539138323f,0.881921264f,0.471396737f,
0.831469612f,0.555570233f,0.831469612f,0.555570233f,0.820401444f,0.57178796f,0.773010453f,0.634393284f,0.809016994f,0.587785252f,1.f,0.f,
0.797320654f,0.603555942f,0.995184727f,0.0980171403f,0.785316931f,0.619093949f,0.98078528f,0.195090322f,0.773010453f,0.634393284f,0.956940336f,0.290284677f,
0.760405966f,0.649448048f,0.923879533f,0.382683432f,0.747508327f,0.664252438f,0.881921264f,0.471396737f,0.734322509f,0.678800746f,0.831469612f,0.555570233f,
0.720853597f,0.693087363f,0.773010453f,0.634393284f,0.707106781f,0.707106781f,1.f,0.f,0.693087363f,0.720853597f,0.995184727f,0.0980171403f,
0.678800746f,0.734322509f,0.98078528f,0.195090322f,0.664252438f,0.747508327f,0.956940336f,0.290284677f,0.649448048f,0.760405966f,0.923879533f,0.382683432f,
0.634393284f,0.773010453f,0.881921264f,0.471396737f,0.619093949f,0.785316931f,0.831469612f,0.555570233f,0.603555942f,0.797320654f,0.773010453f,0.634393284f,
0.587785252f,0.809016994f,1.f,0.f,0.57178796f,0.820401444f,0.995184727f,0.0980171403f,0.555570233f,0.831469612f,0.98078528f,0.195090322f,
0.539138323f,0.842217234f,0.956940336f,0.290284677f,0.522498565f,0.852640164f,0.923879533f,0.382683432f,0.505657373f,0.862734386f,0.881921264f,0.471396737f,
0.488621241f,0.872496007f,0.831469612f,0.555570233f,0.471396737f,0.881921264f,0.773010453f,0.634393284f,0.4539905f,0.891006524f,1.f,0.f,
0.436409241f,0.899748284f,0.995184727f,0.0980171403f,0.418659738f,0.908143174f,0.98078528f,0.195090322f,0.400748833f,0.916187957f,0.956940336f,0.290284677f,
0.382683432f,0.923879533f,0.923879533f,0.382683432f,0.3644705f,0.931214935f,0.881921264f,0.471396737f,0.346117057f,0.938191336f,0.831469612f,0.555570233f,
0.32763018f,0.944806046f,0.773010453f,0.634393284f,1.f,0.f,1.f,0.f,1.f,0.f,1.f,0.f,
1.f,0.f,0.995184727f,0.0980171403f,1.f,0.f,0.995184727f,0.0980171403f,1.f,0.f,0.98078528f,0.195090322f,
1.f,0.f,0.98078528f,0.195090322f,1.f,0.f,0.956940336f,0.290284677f,1.f,0.f,0.956940336f,0.290284677f,
0.999698819f,0.0245412285f,0.923879533f,0.382683432f,0.999698819f,0.0245412285f,0.923879533f,0.382683432f,0.999698819f,0.0245412285f,0.881921264f,0.471396737f,
0.999698819f,0.0245412285f,0.881921264f,0.471396737f,0.999698819f,0.0245412285f,0.831469612f,0.555570233f,0.999698819f,0.0245412285f,0.831469612f,0.555570233f,
0.999698819f,0.0245412285f,0.773010453f,0.634393284f,0.999698819f,0.0245412285f,0.773010453f,0.634393284f,0.998795456f,0.0490676743f,1.f,0.f,
0.998795456f,0.0490676743f,1.f,0.f,0.998795456f,0.0490676743f,0.995184727f,0.0980171403f,0.998795456f,0.0490676743f,0.995184727f,0.0980171403f,
0.998795456f,0.0490676743f,0.98078528f,0.195090322f,0.998795456f,0.0490676743f,0.98078528f,0.195090322f,0.998795456f,0.0490676743f,0.956940336f,0.290284677f,
0.998795456f,0.0490676743f,0.956940336f,0.290284677f,0.997290457f,0.0735645636f,0.923879533f,0.382683432f,0.997290457f,0.0735645636f,0.923879533f,0.382683432f,
0.997290457f,0.0735645636f,0.881921264f,0.471396737f,0.997290457f,0.0735645636f,0.881921264f,0.471396737f,0.997290457f,0.0735645636f,0.831469612f,0.555570233f,
0.997290457f,0.0735645636f,0.831469612f,0.555570233f,0.997290457f,0.0735645636f,0.773010453f,0.634393284f,0.997290457f,0.0735645636f,0.773010453f,0.634393284f,
0.995184727f,0.0980171403f,1.f,0.f,0.995184727f,0.0980171403f,1.f,0.f,0.995184727f,0.0980171403f,0.995184727f,0.0980171403f,
0.995184727f,0.0980171403f,0.995184727f,0.0980171403f,0.995184727f,0.0980171403f,0.98078528f,0.195090322f,0.995184727f,0.0980171403f,0.98078528f,0.195090322f,
0.995184727f,0.0980171403f,0.956940336f,0.290284677f,0.995184727f,0.0980171403f,0.956940336f,0.290284677f,0.992479535f,0.122410675f,0.923879533f,0.382683432f,
0.992479535f,0.122410675f,0.923879533f,0.382683432f,0.992479535f,0.122410675f,0.881921264f,0.471396737f,0.992479535f,0.122410675f,0.881921264f,0.471396737f,
0.992479535f,0.122410675f,0.831469612f,0.555570233f,0.992479535f,0.122410675f,0.831469612f,0.555570233f,0.992479535f,0.122410675f,0.773010453f,0.634393284f,
0.992479535f,0.122410675f,0.773010453f,0.634393284f,0.98917651f,0.146730474f,1.f,0.f,0.98917651f,0.146730474f,1.f,0.f,
0.98917651f,0.146730474f,0.995184727f,0.0980171403f,0.98917651f,0.146730474f,0.995184727f,0.0980171403f,0.98917651f,0.146730474f,0.98078528f,0.195090322f,
0.98917651f,0.146730474f,0.98078528f,0.195090322f,0.98917651f,0.146730474f,0.956940336f,0.290284677f,0.98917651f,0.146730474f,0.956940336f,0.290284677f,
0.985277642f,0.170961889f,0.923879533f,0.382683432f,0.985277642f,0.170961889f,0.923879533f,0.382683432f,0.985277642f,0.170961889f,0.881921264f,0.471396737f,
0.985277642f,0.170961889f,0.881921264f,0.471396737f,0.985277642f,0.170961889f,0.831469612f,0.555570233f,0.985277642f,0.170961889f,0.831469612f,0.555570233f,
0.985277642f,0.170961889f,0.773010453f,0.634393284f,0.985277642f,0.170961889f,0.773010453f,0.634393284f,0.98078528f,0.195090322f,1.f,0.f,
0.98078528f,0.195090322f,1.f,0.f,0.98078528f,0.195090322f,0.995184727f,0.0980171403f,0.98078528f,0.195090322f,0.995184727f,0.0980171403f,
0.98078528f,0.195090322f,0.98078528f,0.195090322f,0.98078528f,0.195090322f,0.98078528f,0.195090322f,0.98078528f,0.195090322f,0.956940336f,0.290284677f,
0.98078528f,0.195090322f,0.956940336f,0.290284677f,0.97570213f,0.21910124f,0.923879533f,0.382683432f,0.97570213f,0.21910124f,0.923879533f,0.382683432f,
0.97570213f,0.21910124f,0.881921264f,0.471396737f,0.97570213f,0.21910124f,0.881921264f,0.471396737f,0.97570213f,0.21910124f,0.831469612f,0.555570233f,
0.97570213f,0.21910124f,0.831469612f,0.555570233f,0.97570213f,0.21910124f,0.773010453f,0.634393284f,0.97570213f,0.21910124f,0.773010453f,0.634393284f,
0.970031253f,0.24298018f,1.f,0.f,0.970031253f,0.24298018f,1.f,0.f,0.970031253f,0.24298018f,0.995184727f,0.0980171403f,
0.970031253f,0.24298018f,0.995184727f,0.0980171403f,0.970031253f,0.24298018f,0.98078528f,0.195090322f,0.970031253f,0.24298018f,0.98078528f,0.195090322f,
0.970031253f,0.24298018f,0.956940336f,0.290284677f,0.970031253f,0.24298018f,0.956940336f,0.290284677f,0.963776066f,0.266712757f,0.923879533f,0.382683432f,
0.963776066f,0.266712757f,0.923879533f,0.382683432f,0.963776066f,0.266712757f,0.881921264f,0.471396737f,0.963776066f,0.266712757f,0.881921264f,0.471396737f,
0.963776066f,0.266712757f,0.831469612f,0.555570233f,0.963776066f,0.266712757f,0.831469612f,0.555570233f,0.963776066f,0.266712757f,0.773010453f,0.634393284f,
0.963776066f,0.266712757f,0.773010453f,0.634393284f,0.956940336f,0.290284677f,1.f,0.f,0.956940336f,0.290284677f,1.f,0.f,
0.956940336f,0.290284677f,0.995184727f,0.0980171403f,0.956940336f,0.290284677f,0.995184727f,0.0980171403f,0.956940336f,0.290284677f,0.98078528f,0.195090322f,
0.956940336f,0.290284677f,0.98078528f,0.195090322f,0.956940336f,0.290284677f,0.956940336f,0.290284677f,0.956940336f,0.290284677f,0.956940336f,0.290284677f,
0.949528181f,0.31368174f,0.923879533f,0.382683432f,0.949528181f,0.31368174f,0.923879533f,0.382683432f,0.949528181f,0.31368174f,0.881921264f,0.471396737f,
0.949528181f,0.31368174f,0.881921264f,0.471396737f,0.949528181f,0.31368174f,0.831469612f,0.555570233f,0.949528181f,0.31368174f,0.831469612f,0.555570233f,
0.949528181f,0.31368174f,0.773010453f,0.634393284f,0.949528181f,0.31368174f,0.773010453f,0.634393284f,0.941544065f,0.336889853f,1.f,0.f,
0.941544065f,0.336889853f,1.f,0.f,0.941544065f,0.336889853f,0.995184727f,0.0980171403f,0.941544065f,0.336889853f,0.995184727f,0.0980171403f,
0.941544065f,0.336889853f,0.98078528f,0.195090322f,0.941544065f,0.336889853f,0.98078528f,0.195090322f,0.941544065f,0.336889853f,0.956940336f,0.290284677f,
0.941544065f,0.336889853f,0.956940336f,0.290284677f,0.932992799f,0.359895037f,0.923879533f,0.382683432f,0.932992799f,0.359895037f,0.923879533f,0.382683432f,
0.932992799f,0.359895037f,0.881921264f,0.471396737f,0.932992799f,0.359895037f,0.881921264f,0.471396737f,0.932992799f,0.359895037f,0.831469612f,0.555570233f,
0.932992799f,0.359895037f,0.831469612f,0.555570233f,0.932992799f,0.359895037f,0.773010453f,0.634393284f,0.932992799f,0.359895037f,0.773010453f,0.634393284f,
0.923879533f,0.382683432f,1.f,0.f,0.923879533f,0.382683432f,1.f,0.f,0.923879533f,0.382683432f,0.995184727f,0.0980171403f,
0.923879533f,0.382683432f,0.995184727f,0.0980171403f,0.923879533f,0.382683432f,0.98078528f,0.195090322f,0.923879533f,0.382683432f,0.98078528f,0.195090322f,
0.923879533f,0.382683432f,0.956940336f,0.290284677f,0.923879533f,0.382683432f,0.956940336f,0.290284677f,0.914209756f,0.405241314f,0.923879533f,0.382683432f,
0.914209756f,0.405241314f,0.923879533f,0.382683432f,0.914209756f,0.405241314f,0.881921264f,0.471396737f,0.914209756f,0.405241314f,0.881921264f,0.471396737f,
0.914209756f,0.405241314f,0.831469612f,0.555570233f,0.914209756f,0.405241314f,0.831469612f,0.555570233f,0.914209756f,0.405241314f,0.773010453f,0.634393284f,
0.914209756f,0.405241314f,0.773010453f,0.634393284f,0.903989293f,0.427555093f,1.f,0.f,0.903989293f,0.427555093f,1.f,0.f,
0.903989293f,0.427555093f,0.995184727f,0.0980171403f,0.903989293f,0.427555093f,0.995184727f,0.0980171403f,0.903989293f,0.427555093f,0.98078528f,0.195090322f,
0.903989293f,0.427555093f,0.98078528f,0.195090322f,0.903989293f,0.427555093f,0.956940336f,0.290284677f,0.903989293f,0.427555093f,0.956940336f,0.290284677f,
0.893224301f,0.44961133f,0.923879533f,0.382683432f,0.893224301f,0.44961133f,0.923879533f,0.382683432f,0.893224301f,0.44961133f,0.881921264f,0.471396737f,
0.893224301f,0.44961133f,0.881921264f,0.471396737f,0.893224301f,0.44961133f,0.831469612f,0.555570233f,0.893224301f,0.44961133f,0.831469612f,0.555570233f,
0.893224301f,0.44961133f,0.773010453f,0.634393284f,0.893224301f,0.44961133f,0.773010453f,0.634393284f,0.881921264f,0.471396737f,1.f,0.f,
0.881921264f,0.471396737f,1.f,0.f,0.881921264f,0.471396737f,0.995184727f,0.0980171403f,0.881921264f,0.471396737f,0.995184727f,0.0980171403f,
0.881921264f,0.471396737f,0.98078528f,0.195090322f,0.881921264f,0.471396737f,0.98078528f,0.195090322f,0.881921264f,0.471396737f,0.956940336f,0.290284677f,
0.881921264f,0.471396737f,0.956940336f,0.290284677f,0.870086991f,0.492898192f,0.923879533f,0.382683432f,0.870086991f,0.492898192f,0.923879533f,0.382683432f,
0.870086991f,0.492898192f,0.881921264f,0.471396737f,0.870086991f,0.492898192f,0.881921264f,0.471396737f,0.870086991f,0.492898192f,0.831469612f,0.555570233f,
0.870086991f,0.492898192f,0.831469612f,0.555570233f,0.870086991f,0.492898192f,0.773010453f,0.634393284f,0.870086991f,0.492898192f,0.773010453f,0.634393284f,
0.85772861f,0.514102744f,1.f,0.f,0.85772861f,0.514102744f,1.f,0.f,0.85772861f,0.514102744f,0.995184727f,0.0980171403f,
0.85772861f,0.514102744f,0.995184727f,0.0980171403f,0.85772861f,0.514102744f,0.98078528f,0.195090322f,0.85772861f,0.514102744f,0.98078528f,0.195090322f,
0.85772861f,0.514102744f,0.956940336f,0.290284677f,0.85772861f,0.514102744f,0.956940336f,0.290284677f,0.844853565f,0.53499762f,0.923879533f,0.382683432f,
0.844853565f,0.53499762f,0.923879533f,0.382683432f,0.844853565f,0.53499762f,0.881921264f,0.471396737f,0.844853565f,0.53499762f,0.881921264f,0.471396737f,
0.844853565f,0.53499762f,0.831469612f,0.555570233f,0.844853565f,0.53499762f,0.831469612f,0.555570233f,0.844853565f,0.53499762f,0.773010453f,0.634393284f,
0.844853565f,0.53499762f,0.773010453f,0.634393284f,0.831469612f,0.555570233f,1.f,0.f,0.831469612f,0.555570233f,1.f,0.f,
0.831469612f,0.555570233f,0.995184727f,0.0980171403f,0.831469612f,0.555570233f,0.995184727f,0.0980171403f,0.831469612f,0.555570233f,0.98078528f,0.195090322f,
0.831469612f,0.555570233f,0.98078528f,0.195090322f,0.831469612f,0.555570233f,0.956940336f,0.290284677f,0.831469612f,0.555570233f,0.956940336f,0.290284677f,
0.817584813f,0.575808191f,0.923879533f,0.382683432f,0.817584813f,0.575808191f,0.923879533f,0.382683432f,0.817584813f,0.575808191f,0.881921264f,0.471396737f,
0.817584813f,0.575808191f,0.881921264f,0.471396737f,0.817584813f,0.575808191f,0.831469612f,0.555570233f,0.817584813f,0.575808191f,0.831469612f,0.555570233f,
0.817584813f,0.575808191f,0.773010453f,0.634393284f,0.817584813f,0.575808191f,0.773010453f,0.634393284f,0.803207531f,0.595699304f,1.f,0.f,
0.803207531f,0.595699304f,1.f,0.f,0.803207531f,0.595699304f,0.995184727f,0.0980171403f,0.803207531f,0.595699304f,0.995184727f,0.0980171403f,
0.803207531f,0.595699304f,0.98078528f,0.195090322f,0.803207531f,0.595699304f,0.98078528f,0.195090322f,0.803207531f,0.595699304f,0.956940336f,0.290284677f,
0.803207531f,0.595699304f,0.956940336f,0.290284677f,0.788346428f,0.615231591f,0.923879533f,0.382683432f,0.788346428f,0.615231591f,0.923879533f,0.382683432f,
0.788346428f,0.615231591f,0.881921264f,0.471396737f,0.788346428f,0.615231591f,0.881921264f,0.471396737f,0.788346428f,0.615231591f,0.831469612f,0.555570233f,
0.788346428f,0.615231591f,0.831469612f,0.555570233f,0.788346428f,0.615231591f,0.773010453f,0.634393284f,0.788346428f,0.615231591f,0.773010453f,0.634393284f,
0.773010453f,0.634393284f,1.f,0.f,0.773010453f,0.634393284f,1.f,0.f,0.773010453f,0.634393284f,0.995184727f,0.0980171403f,
0.773010453f,0.634393284f,0.995184727f,0.0980171403f,0.773010453f,0.634393284f,0.98078528f,0.195090322f,0.773010453f,0.634393284f,0.98078528f,0.195090322f,
0.773010453f,0.634393284f,0.956940336f,0.290284677f,0.773010453f,0.634393284f,0.956940336f,0.290284677f,0.757208847f,0.653172843f,0.923879533f,0.382683432f,
0.757208847f,0.653172843f,0.923879533f,0.382683432f,0.757208847f,0.653172843f,0.881921264f,0.471396737f,0.757208847f,0.653172843f,0.881921264f,0.471396737f,
0.757208847f,0.653172843f,0.831469612f,0.555570233f,0.757208847f,0.653172843f,0.831469612f,0.555570233f,0.757208847f,0.653172843f,0.773010453f,0.634393284f,
0.757208847f,0.653172843f,0.773010453f,0.634393284f,0.740951125f,0.671558955f,1.f,0.f,0.740951125f,0.671558955f,1.f,0.f,
0.740951125f,0.671558955f,0.995184727f,0.0980171403f,0.740951125f,0.671558955f,0.995184727f,0.0980171403f,0.740951125f,0.671558955f,0.98078528f,0.195090322f,
0.740951125f,0.671558955f,0.98078528f,0.195090322f,0.740951125f,0.671558955f,0.956940336f,0.290284677f,0.740951125f,0.671558955f,0.956940336f,0.290284677f,
0.724247083f,0.689540545f,0.923879533f,0.382683432f,0.724247083f,0.689540545f,0.923879533f,0.382683432f,0.724247083f,0.689540545f,0.881921264f,0.471396737f,
0.724247083f,0.689540545f,0.881921264f,0.471396737f,0.724247083f,0.689540545f,0.831469612f,0.555570233f,0.724247083f,0.689540545f,0.831469612f,0.555570233f,
0.724247083f,0.689540545f,0.773010453f,0.634393284f,0.724247083f,0.689540545f,0.773010453f,0.634393284f,0.707106781f,0.707106781f,1.f,0.f,
0.707106781f,0.707106781f,1.f,0.f,0.707106781f,0.707106781f,0.995184727f,0.0980171403f,0.707106781f,0.707106781f,0.995184727f,0.0980171403f,
0.707106781f,0.707106781f,0.98078528f,0.195090322f,0.707106781f,0.707106781f,0.98078528f,0.195090322f,0.707106781f,0.707106781f,0.956940336f,0.290284677f,
0.707106781f,0.707106781f,0.956940336f,0.290284677f,0.689540545f,0.724247083f,0.923879533f,0.382683432f,0.689540545f,0.724247083f,0.923879533f,0.382683432f,
0.689540545f,0.724247083f,0.881921264f,0.471396737f,0.689540545f,0.724247083f,0.881921264f,0.471396737f,0.689540545f,0.724247083f,0.831469612f,0.555570233f,
0.689540545f,0.724247083f,0.831469612f,0.555570233f,0.689540545f,0.724247083f,0.773010453f,0.634393284f,0.689540545f,0.724247083f,0.773010453f,0.634393284f,
0.671558955f,0.740951125f,1.f,0.f,0.671558955f,0.740951125f,1.f,0.f,0.671558955f,0.740951125f,0.995184727f,0.0980171403f,
0.671558955f,0.740951125f,0.995184727f,0.0980171403f,0.671558955f,0.740951125f,0.98078528f,0.195090322f,0.671558955f,0.740951125f,0.98078528f,0.195090322f,
0.671558955f,0.740951125f,0.956940336f,0.290284677f,0.671558955f,0.740951125f,0.956940336f,0.290284677f,0.653172843f,0.757208847f,0.923879533f,0.382683432f,
0.653172843f,0.757208847f,0.923879533f,0.382683432f,0.653172843f,0.757208847f,0.881921264f,0.471396737f,0.653172843f,0.757208847f,0.881921264f,0.471396737f,
0.653172843f,0.757208847f,0.831469612f,0.555570233f,0.653172843f,0.757208847f,0.831469612f,0.555570233f,0.653172843f,0.757208847f,0.773010453f,0.634393284f,
0.653172843f,0.757208847f,0.773010453f,0.634393284f,0.634393284f,0.773010453f,1.f,0.f,0.634393284f,0.773010453f,1.f,0.f,
0.634393284f,0.773010453f,0.995184727f,0.0980171403f,0.634393284f,0.773010453f,0.995184727f,0.0980171403f,0.634393284f,0.773010453f,0.98078528f,0.195090322f,
0.634393284f,0.773010453f,0.98078528f,0.195090322f,0.634393284f,0.773010453f,0.956940336f,0.290284677f,0.634393284f,0.773010453f,0.956940336f,0.290284677f,
0.615231591f,0.788346428f,0.923879533f,0.382683432f,0.615231591f,0.788346428f,0.923879533f,0.382683432f,0.615231591f,0.788346428f,0.881921264f,0.471396737f,
0.615231591f,0.788346428f,0.881921264f,0.471396737f,0.615231591f,0.788346428f,0.831469612f,0.555570233f,0.615231591f,0.788346428f,0.831469612f,0.555570233f,
0.615231591f,0.788346428f,0.773010453f,0.634393284f,0.615231591f,0.788346428f,0.773010453f,0.634393284f,0.595699304f,0.803207531f,1.f,0.f,
0.595699304f,0.803207531f,1.f,0.f,0.595699304f,0.803207531f,0.995184727f,0.0980171403f,0.595699304f,0.803207531f,0.995184727f,0.0980171403f,
0.595699304f,0.803207531f,0.98078528f,0.195090322f,0.595699304f,0.803207531f,0.98078528f,0.195090322f,0.595699304f,0.803207531f,0.956940336f,0.290284677f,
0.595699304f,0.803207531f,0.956940336f,0.290284677f,0.575808191f,0.817584813f,0.923879533f,0.382683432f,0.575808191f,0.817584813f,0.923879533f,0.382683432f,
0.575808191f,0.817584813f,0.881921264f,0.471396737f,0.575808191f,0.817584813f,0.881921264f,0.471396737f,0.575808191f,0.817584813f,0.831469612f,0.555570233f,
0.575808191f,0.817584813f,0.831469612f,0.555570233f,0.575808191f,0.817584813f,0.773010453f,0.634393284f,0.575808191f,0.817584813f,0.773010453f,0.634393284f,
0.555570233f,0.831469612f,1.f,0.f,0.555570233f,0.831469612f,1.f,0.f,0.555570233f,0.831469612f,0.995184727f,0.0980171403f,
0.555570233f,0.831469612f,0.995184727f,0.0980171403f,0.555570233f,0.831469612f,0.98078528f,0.195090322f,0.555570233f,0.831469612f,0.98078528f,0.195090322f,
0.555570233f,0.831469612f,0.956940336f,0.290284677f,0.555570233f,0.831469612f,0.956940336f,0.290284677f,0.53499762f,0.844853565f,0.923879533f,0.382683432f,
0.53499762f,0.844853565f,0.923879533f,0.382683432f,0.53499762f,0.844853565f,0.881921264f,0.471396737f,0.53499762f,0.844853565f,0.881921264f,0.471396737f,
0.53499762f,0.844853565f,0.831469612f,0.555570233f,0.53499762f,0.844853565f,0.831469612f,0.555570233f,0.53499762f,0.844853565f,0.773010453f,0.634393284f,
0.53499762f,0.844853565f,0.773010453f,0.634393284f,0.514102744f,0.85772861f,1.f,0.f,0.514102744f,0.85772861f,1.f,0.f,
0.514102744f,0.85772861f,0.995184727f,0.0980171403f,0.514102744f,0.85772861f,0.995184727f,0.0980171403f,0.514102744f,0.85772861f,0.98078528f,0.195090322f,
0.514102744f,0.85772861f,0.98078528f,0.195090322f,0.514102744f,0.85772861f,0.956940336f,0.290284677f,0.514102744f,0.85772861f,0.956940336f,0.290284677f,
0.492898192f,0.870086991f,0.923879533f,0.382683432f,0.492898192f,0.870086991f,0.923879533f,0.382683432f,0.492898192f,0.870086991f,0.881921264f,0.471396737f,
0.492898192f,0.870086991f,0.881921264f,0.471396737f,0.492898192f,0.870086991f,0.831469612f,0.555570233f,0.492898192f,0.870086991f,0.831469612f,0.555570233f,
0.492898192f,0.870086991f,0.773010453f,0.634393284f,0.492898192f,0.870086991f,0.773010453f,0.634393284f,0.471396737f,0.881921264f,1.f,0.f,
0.471396737f,0.881921264f,1.f,0.f,0.471396737f,0.881921264f,0.995184727f,0.0980171403f,0.471396737f,0.881921264f,0.995184727f,0.0980171403f,
0.471396737f,0.881921264f,0.98078528f,0.195090322f,0.471396737f,0.881921264f,0.98078528f,0.195090322f,0.471396737f,0.881921264f,0.956940336f,0.290284677f,
0.471396737f,0.881921264f,0.956940336f,0.290284677f,0.44961133f,0.893224301f,0.923879533f,0.382683432f,0.44961133f,0.893224301f,0.923879533f,0.382683432f,
0.44961133f,0.893224301f,0.881921264f,0.471396737f,0.44961133f,0.893224301f,0.881921264f,0.471396737f,0.44961133f,0.893224301f,0.831469612f,0.555570233f,
0.44961133f,0.893224301f,0.831469612f,0.555570233f,0.44961133f,0.893224301f,0.773010453f,0.634393284f,0.44961133f,0.893224301f,0.773010453f,0.634393284f,
0.427555093f,0.903989293f,1.f,0.f,0.427555093f,0.903989293f,1.f,0.f,0.427555093f,0.903989293f,0.995184727f,0.0980171403f,
0.427555093f,0.903989293f,0.995184727f,0.0980171403f,0.427555093f,0.903989293f,0.98078528f,0.195090322f,0.427555093f,0.903989293f,0.98078528f,0.195090322f,
0.427555093f,0.903989293f,0.956940336f,0.290284677f,0.427555093f,0.903989293f,0.956940336f,0.290284677f,0.405241314f,0.914209756f,0.923879533f,0.382683432f,
0.405241314f,0.914209756f,0.923879533f,0.382683432f,0.405241314f,0.914209756f,0.881921264f,0.471396737f,0.405241314f,0.914209756f,0.881921264f,0.471396737f,
0.405241314f,0.914209756f,0.831469612f,0.555570233f,0.405241314f,0.914209756f,0.831469612f,0.555570233f,0.405241314f,0.914209756f,0.773010453f,0.634393284f,
0.405241314f,0.914209756f,0.773010453f,0.634393284f,0.382683432f,0.923879533f,1.f,0.f,0.382683432f,0.923879533f,1.f,0.f,
0.382683432f,0.923879533f,0.995184727f,0.0980171403f,0.382683432f,0.923879533f,0.995184727f,0.0980171403f,0.382683432f,0.923879533f,0.98078528f,0.195090322f,
0.382683432f,0.923879533f,0.98078528f,0.195090322f,0.382683432f,0.923879533f,0.956940336f,0.290284677f,0.382683432f,0.923879533f,0.956940336f,0.290284677f,
0.359895037f,0.932992799f,0.923879533f,0.382683432f,0.359895037f,0.932992799f,0.923879533f,0.382683432f,0.359895037f,0.932992799f,0.881921264f,0.471396737f,
0.359895037f,0.932992799f,0.881921264f,0.471396737f,0.359895037f,0.932992799f,0.831469612f,0.555570233f,0.359895037f,0.932992799f,0.831469612f,0.555570233f,
0.359895037f,0.932992799f,0.773010453f,0.634393284f,0.359895037f,0.932992799f,0.773010453f,0.634393284f,0.336889853f,0.941544065f,1.f,0.f,
0.336889853f,0.941544065f,1.f,0.f,0.336889853f,0.941544065f,0.995184727f,0.0980171403f,0.336889853f,0.941544065f,0.995184727f,0.0980171403f,
0.336889853f,0.941544065f,0.98078528f,0.195090322f,0.336889853f,0.941544065f,0.98078528f,0.195090322f,0.336889853f,0.941544065f,0.956940336f,0.290284677f,
0.336889853f,0.941544065f,0.956940336f,0.290284677f,0.31368174f,0.949528181f,0.923879533f,0.382683432f,0.31368174f,0.949528181f,0.923879533f,0.382683432f,
0.31368174f,0.949528181f,0.881921264f,0.471396737f,0.31368174f,0.949528181f,0.881921264f,0.471396737f,0.31368174f,0.949528181f,0.831469612f,0.555570233f,
0.31368174f,0.949528181f,0.831469612f,0.555570233f,0.31368174f,0.949528181f,0.773010453f,0.634393284f,0.31368174f,0.949528181f,0.773010453f,0.634393284f,
0.290284677f,0.956940336f,1.f,0.f,0.290284677f,0.956940336f,1.f,0.f,0.290284677f,0.956940336f,0.995184727f,0.0980171403f,
0.290284677f,0.956940336f,0.995184727f,0.0980171403f,0.290284677f,0.956940336f,0.98078528f,0.195090322f,0.290284677f,0.956940336f,0.98078528f,0.195090322f,
0.290284677f,0.956940336f,0.956940336f,0.290284677f,0.290284677f,0.956940336f,0.956940336f,0.290284677f,0.266712757f,0.963776066f,0.923879533f,0.382683432f,
0.266712757f,0.963776066f,0.923879533f,0.382683432f,0.266712757f,0.963776066f,0.881921264f,0.471396737f,0.266712757f,0.963776066f,0.881921264f,0.471396737f,
0.266712757f,0.963776066f,0.831469612f,0.555570233f,0.266712757f,0.963776066f,0.831469612f,0.555570233f,0.266712757f,0.963776066f,0.773010453f,0.634393284f,
0.266712757f,0.963776066f,0.773010453f,0.634393284f,0.24298018f,0.970031253f,1.f,0.f,0.24298018f,0.970031253f,1.f,0.f,
0.24298018f,0.970031253f,0.995184727f,0.0980171403f,0.24298018f,0.970031253f,0.995184727f,0.0980171403f,0.24298018f,0.970031253f,0.98078528f,0.195090322f,
0.24298018f,0.970031253f,0.98078528f,0.195090322f,0.24298018f,0.970031253f,0.956940336f,0.290284677f,0.24298018f,0.970031253f,0.956940336f,0.290284677f,
0.21910124f,0.97570213f,0.923879533f,0.382683432f,0.21910124f,0.97570213f,0.923879533f,0.382683432f,0.21910124f,0.97570213f,0.881921264f,0.471396737f,
0.21910124f,0.97570213f,0.881921264f,0.471396737f,0.21910124f,0.97570213f,0.831469612f,0.555570233f,0.21910124f,0.97570213f,0.831469612f,0.555570233f,
0.21910124f,0.97570213f,0.773010453f,0.634393284f,0.21910124f,0.97570213f,0.773010453f,0.634393284f,0.195090322f,0.98078528f,1.f,0.f,
0.195090322f,0.98078528f,1.f,0.f,0.195090322f,0.98078528f,0.995184727f,0.0980171403f,0.195090322f,0.98078528f,0.995184727f,0.0980171403f,
0.195090322f,0.98078528f,0.98078528f,0.195090322f,0.195090322f,0.98078528f,0.98078528f,0.195090322f,0.195090322f,0.98078528f,0.956940336f,0.290284677f,
0.195090322f,0.98078528f,0.956940336f,0.290284677f,0.170961889f,0.985277642f,0.923879533f,0.382683432f,0.170961889f,0.985277642f,0.923879533f,0.382683432f,
0.170961889f,0.985277642f,0.881921264f,0.471396737f,0.170961889f,0.985277642f,0.881921264f,0.471396737f,0.170961889f,0.985277642f,0.831469612f,0.555570233f,
0.170961889f,0.985277642f,0.831469612f,0.555570233f,0.170961889f,0.985277642f,0.773010453f,0.634393284f,0.170961889f,0.985277642f,0.773010453f,0.634393284f,
0.146730474f,0.98917651f,1.f,0.f,0.146730474f,0.98917651f,1.f,0.f,0.146730474f,0.98917651f,0.995184727f,0.0980171403f,
0.146730474f,0.98917651f,0.995184727f,0.0980171403f,0.146730474f,0.98917651f,0.98078528f,0.195090322f,0.146730474f,0.98917651f,0.98078528f,0.195090322f,
0.146730474f,0.98917651f,0.956940336f,0.290284677f,0.146730474f,0.98917651f,0.956940336f,0.290284677f,0.122410675f,0.992479535f,0.923879533f,0.382683432f,
0.122410675f,0.992479535f,0.923879533f,0.382683432f,0.122410675f,0.992479535f,0.881921264f,0.471396737f,0.122410675f,0.992479535f,0.881921264f,0.471396737f,
0.122410675f,0.992479535f,0.831469612f,0.555570233f,0.122410675f,0.992479535f,0.831469612f,0.555570233f,0.122410675f,0.992479535f,0.773010453f,0.634393284f,
0.122410675f,0.992479535f,0.773010453f,0.634393284f,0.0980171403f,0.995184727f,1.f,0.f,0.0980171403f,0.995184727f,1.f,0.f,
0.0980171403f,0.995184727f,0.995184727f,0.0980171403f,0.0980171403f,0.995184727f,0.995184727f,0.0980171403f,0.0980171403f,0.995184727f,0.98078528f,0.195090322f,
0.0980171403f,0.995184727f,0.98078528f,0.195090322f,0.0980171403f,0.995184727f,0.956940336f,0.290284677f,0.0980171403f,0.995184727f,0.956940336f,0.290284677f,
0.0735645636f,0.997290457f,0.923879533f,0.382683432f,0.0735645636f,0.997290457f,0.923879533f,0.382683432f,0.0735645636f,0.997290457f,0.881921264f,0.471396737f,
0.0735645636f,0.997290457f,0.881921264f,0.471396737f,0.0735645636f,0.997290457f,0.831469612f,0.555570233f,0.0735645636f,0.997290457f,0.831469612f,0.555570233f,
0.0735645636f,0.997290457f,0.773010453f,0.634393284f,0.0735645636f,0.997290457f,0.773010453f,0.634393284f,0.0490676743f,0.998795456f,1.f,0.f,
0.0490676743f,0.998795456f,1.f,0.f,0.0490676743f,0.998795456f,0.995184727f,0.0980171403f,0.0490676743f,0.998795456f,0.995184727f,0.0980171403f,
0.0490676743f,0.998795456f,0.98078528f,0.195090322f,0.0490676743f,0.998795456f,0.98078528f,0.195090322f,0.0490676743f,0.998795456f,0.956940336f,0.290284677f,
0.0490676743f,0.998795456f,0.956940336f,0.290284677f,0.0245412285f,0.999698819f,0.923879533f,0.382683432f,0.0245412285f,0.999698819f,0.923879533f,0.382683432f,
0.0245412285f,0.999698819f,0.881921264f,0.471396737f,0.0245412285f,0.999698819f,0.881921264f,0.471396737f,0.0245412285f,0.999698819f,0.831469612f,0.555570233f,
0.0245412285f,0.999698819f,0.831469612f,0.555570233f,0.0245412285f,0.999698819f,0.773010453f,0.634393284f,0.0245412285f,0.999698819f,0.773010453f,0.634393284f,
1.f,0.f,0.999951809f,0.00981731934f,0.99980724f,0.0196336925f,0.999566309f,0.0294481732f,0.999229036f,0.0392598158f,0.998795456f,0.0490676743f,
0.99826561f,0.0588708037f,0.997639549f,0.0686682589f,0.996917334f,0.0784590957f,0.996099033f,0.0882423705f,0.995184727f,0.0980171403f,0.994174502f,0.107782463f,
0.993068457f,0.117537397f,0.991866698f,0.127281003f,0.99056934f,0.137012342f,0.98917651f,0.146730474f,0.987688341f,0.156434465f,0.986104976f,0.166123378f,
0.984426568f,0.17579628f,0.982653279f,0.185452238f,0.98078528f,0.195090322f,0.978822751f,0.204709603f,0.976765881f,0.214309153f,0.974614869f,0.223888048f,
0.97236992f,0.233445364f,0.970031253f,0.24298018f,0.967599092f,0.252491577f,0.965073672f,0.261978638f,0.962455236f,0.27144045f,0.959744037f,0.280876099f,
0.956940336f,0.290284677f,0.954044402f,0.299665277f,0.951056516f,0.309016994f,0.947976965f,0.318338928f,0.944806046f,0.32763018f,0.941544065f,0.336889853f,
0.938191336f,0.346117057f,0.934748182f,0.355310901f,0.931214935f,0.3644705f,0.927591935f,0.37359497f,0.923879533f,0.382683432f,0.920078084f,0.391735011f,
0.916187957f,0.400748833f,0.912209526f,0.40972403f,0.908143174f,0.418659738f,0.903989293f,0.427555093f,0.899748284f,0.436409241f,0.895420555f,0.445221326f,
0.891006524f,0.4539905f,0.886506616f,0.462715917f,0.881921264f,0.471396737f,0.877250911f,0.480032122f,0.872496007f,0.488621241f,0.86765701f,0.497163266f,
0.862734386f,0.505657373f,0.85772861f,0.514102744f,0.852640164f,0.522498565f,0.847469539f,0.530844026f,0.842217234f,0.539138323f,0.836883753f,0.547380657f,
0.831469612f,0.555570233f,0.825975333f,0.563706262f,0.820401444f,0.57178796f,0.814748483f,0.579814548f,0.809016994f,0.587785252f,0.803207531f,0.595699304f,
0.797320654f,0.603555942f,0.791356929f,0.611354407f,0.785316931f,0.619093949f,0.779201243f,0.626773822f,0.773010453f,0.634393284f,0.76674516f,0.641951603f,
0.760405966f,0.649448048f,0.753993482f,0.656881899f,0.747508327f,0.664252438f,0.740951125f,0.671558955f,0.734322509f,0.678800746f,0.727623118f,0.685977112f,
0.720853597f,0.693087363f,0.714014598f,0.700130812f,0.707106781f,0.707106781f,0.700130812f,0.714014598f,0.693087363f,0.720853597f,0.685977112f,0.727623118f,
0.678800746f,0.734322509f,0.671558955f,0.740951125f,0.664252438f,0.747508327f,0.656881899f,0.753993482f,0.649448048f,0.760405966f,0.641951603f,0.76674516f,
0.634393284f,0.773010453f,0.626773822f,0.779201243f,0.619093949f,0.785316931f,0.611354407f,0.791356929f,0.603555942f,0.797320654f,0.595699304f,0.803207531f,
0.587785252f,0.809016994f,0.579814548f,0.814748483f,0.57178796f,0.820401444f,0.563706262f,0.825975333f,0.555570233f,0.831469612f,0.547380657f,0.836883753f,
0.539138323f,0.842217234f,0.530844026f,0.847469539f,0.522498565f,0.852640164f,0.514102744f,0.85772861f,0.505657373f,0.862734386f,0.497163266f,0.86765701f,
0.488621241f,0.872496007f,0.480032122f,0.877250911f,0.471396737f,0.881921264f,0.462715917f,0.886506616f,0.4539905f,0.891006524f,0.445221326f,0.895420555f,
0.436409241f,0.899748284f,0.427555093f,0.903989293f,0.418659738f,0.908143174f,0.40972403f,0.912209526f,0.400748833f,0.916187957f,0.391735011f,0.920078084f,
0.382683432f,0.923879533f,0.37359497f,0.927591935f,0.3644705f,0.931214935f,0.355310901f,0.934748182f,0.346117057f,0.938191336f,0.336889853f,0.941544065f,
0.32763018f,0.944806046f,0.318338928f,0.947976965f,0.309016994f,0.951056516f,0.299665277f,0.954044402f,0.290284677f,0.956940336f,0.280876099f,0.959744037f,
0.27144045f,0.962455236f,0.261978638f,0.965073672f,0.252491577f,0.967599092f,0.24298018f,0.970031253f,0.233445364f,0.97236992f,0.223888048f,0.974614869f,
0.214309153f,0.976765881f,0.204709603f,0.978822751f,0.195090322f,0.98078528f,0.185452238f,0.982653279f,0.17579628f,0.984426568f,0.166123378f,0.986104976f,
0.156434465f,0.987688341f,0.146730474f,0.98917651f,0.137012342f,0.99056934f,0.127281003f,0.991866698f,0.117537397f,0.993068457f,0.107782463f,0.994174502f,
0.0980171403f,0.995184727f,0.0882423705f,0.996099033f,0.0784590957f,0.996917334f,0.0686682589f,0.997639549f,0.0588708037f,0.99826561f,0.0490676743f,0.998795456f,
0.0392598158f,0.999229036f,0.0294481732f,0.999566309f,0.0196336925f,0.99980724f,0.00981731934f,0.999951809f,0.f,1.f,-0.00981731934f,0.999951809f,
-0.0196336925f,0.99980724f,-0.0294481732f,0.999566309f,-0.0392598158f,0.999229036f,-0.0490676743f,0.998795456f,-0.0588708037f,0.99826561f,-0.0686682589f,0.997639549f,
1.f,0.f,0.999995294f,0.00306795676f,0.999981175f,0.00613588465f,0.999957645f,0.00920375478f,0.999924702f,0.0122715383f,0.999882347f,0.0153392063f,
0.999830582f,0.0184067299f,0.999769405f,0.0214740803f,0.999698819f,0.0245412285f,0.999618822f,0.0276081458f,0.999529418f,0.0306748032f,0.999430605f,0.0337411719f,
0.999322385f,0.0368072229f,0.999204759f,0.0398729276f,0.999077728f,0.0429382569f,0.998941293f,0.0460031821f,0.998795456f,0.0490676743f,0.998640218f,0.0521317047f,
0.998475581f,0.0551952443f,0.998301545f,0.0582582645f,0.998118113f,0.0613207363f,0.997925286f,0.0643826309f,0.997723067f,0.0674439196f,0.997511456f,0.0705045734f,
0.997290457f,0.0735645636f,0.99706007f,0.0766238614f,0.996820299f,0.079682438f,0.996571146f,0.0827402645f,0.996312612f,0.0857973123f,0.996044701f,0.0888535526f,
0.995767414f,0.0919089565f,0.995480755f,0.0949634953f,0.995184727f,0.0980171403f,0.994879331f,0.101069863f,0.994564571f,0.104121634f,0.994240449f,0.107172425f,
0.99390697f,0.110222207f,0.993564136f,0.113270952f,0.993211949f,0.116318631f,0.992850414f,0.119365215f,0.992479535f,0.122410675f,0.992099313f,0.125454983f,
0.991709754f,0.128498111f,0.99131086f,0.131540029f,0.990902635f,0.134580709f,0.990485084f,0.137620122f,0.99005821f,0.140658239f,0.989622017f,0.143695033f,
0.98917651f,0.146730474f,0.988721692f,0.149764535f,0.988257568f,0.152797185f,0.987784142f,0.155828398f,0.987301418f,0.158858143f,0.986809402f,0.161886394f,
0.986308097f,0.16491312f,0.985797509f,0.167938295f,0.985277642f,0.170961889f,0.984748502f,0.173983873f,0.984210092f,0.17700422f,0.983662419f,0.180022901f,
0.983105487f,0.183039888f,0.982539302f,0.186055152f,0.981963869f,0.189068664f,0.981379193f,0.192080397f,0.98078528f,0.195090322f,0.980182136f,0.198098411f,
0.979569766f,0.201104635f,0.978948175f,0.204108966f,0.978317371f,0.207111376f,0.977677358f,0.210111837f,0.977028143f,0.21311032f,0.976369731f,0.216106797f,
0.97570213f,0.21910124f,0.975025345f,0.222093621f,0.974339383f,0.225083911f,0.97364425f,0.228072083f,0.972939952f,0.231058108f,0.972226497f,0.234041959f,
0.971503891f,0.237023606f,0.970772141f,0.240003022f,0.970031253f,0.24298018f,0.969281235f,0.24595505f,0.968522094f,0.248927606f,0.967753837f,0.251897818f,
0.966976471f,0.25486566f,0.966190003f,0.257831102f,0.965394442f,0.260794118f,0.964589793f,0.263754679f,0.963776066f,0.266712757f,0.962953267f,0.269668326f,
0.962121404f,0.272621355f,0.961280486f,0.275571819f,0.960430519f,0.278519689f,0.959571513f,0.281464938f,0.958703475f,0.284407537f,0.957826413f,0.28734746f,
0.956940336f,0.290284677f,0.956045251f,0.293219163f,0.955141168f,0.296150888f,0.954228095f,0.299079826f,0.95330604f,0.302005949f,0.952375013f,0.30492923f,
0.951435021f,0.30784964f,0.950486074f,0.310767153f,0.949528181f,0.31368174f,0.94856135f,0.316593376f,0.947585591f,0.319502031f,0.946600913f,0.322407679f,
0.945607325f,0.325310292f,0.944604837f,0.328209844f,0.943593458f,0.331106306f,0.942573198f,0.333999651f,0.941544065f,0.336889853f,0.940506071f,0.339776884f,
0.939459224f,0.342660717f,0.938403534f,0.345541325f,0.937339012f,0.34841868f,0.936265667f,0.351292756f,0.93518351f,0.354163525f,0.93409255f,0.357030961f,
0.932992799f,0.359895037f,0.931884266f,0.362755724f,0.930766961f,0.365612998f,0.929640896f,0.36846683f,0.92850608f,0.371317194f,0.927362526f,0.374164063f,
0.926210242f,0.37700741f,0.925049241f,0.379847209f,0.923879533f,0.382683432f,0.922701128f,0.385516054f,0.921514039f,0.388345047f,0.920318277f,0.391170384f,
0.919113852f,0.39399204f,0.917900776f,0.396809987f,0.91667906f,0.3996242f,0.915448716f,0.402434651f,0.914209756f,0.405241314f,0.91296219f,0.408044163f,
0.911706032f,0.410843171f,0.910441292f,0.413638312f,0.909167983f,0.41642956f,0.907886116f,0.419216888f,0.906595705f,0.422000271f,0.905296759f,0.424779681f,
0.903989293f,0.427555093f,0.902673318f,0.430326481f,0.901348847f,0.433093819f,0.900015892f,0.43585708f,0.898674466f,0.438616239f,0.897324581f,0.441371269f,
0.89596625f,0.444122145f,0.894599486f,0.44686884f,0.893224301f,0.44961133f,0.891840709f,0.452349587f,0.890448723f,0.455083587f,0.889048356f,0.457813304f,
0.88763962f,0.460538711f,0.88622253f,0.463259784f,0.884797098f,0.465976496f,0.883363339f,0.468688822f,0.881921264f,0.471396737f,0.880470889f,0.474100215f,
0.879012226f,0.47679923f,0.87754529f,0.479493758f,0.876070094f,0.482183772f,0.874586652f,0.484869248f,0.873094978f,0.48755016f,0.871595087f,0.490226483f,
0.870086991f,0.492898192f,0.868570706f,0.495565262f,0.867046246f,0.498227667f,0.865513624f,0.500885383f,0.863972856f,0.503538384f,0.862423956f,0.506186645f,
0.860866939f,0.508830143f,0.859301818f,0.51146885f,0.85772861f,0.514102744f,0.856147328f,0.516731799f,0.854557988f,0.51935599f,0.852960605f,0.521975293f,
0.851355193f,0.524589683f,0.849741768f,0.527199135f,0.848120345f,0.529803625f,0.846490939f,0.532403128f,0.844853565f,0.53499762f,0.84320824f,0.537587076f,
0.841554977f,0.540171473f,0.839893794f,0.542750785f,0.838224706f,0.545324988f,0.836547727f,0.547894059f,0.834862875f,0.550457973f,0.833170165f,0.553016706f,
0.831469612f,0.555570233f,0.829761234f,0.558118531f,0.828045045f,0.560661576f,0.826321063f,0.563199344f,0.824589303f,0.565731811f,0.822849781f,0.568258953f,
0.821102515f,0.570780746f,0.81934752f,0.573297167f,0.817584813f,0.575808191f,0.815814411f,0.578313796f,0.81403633f,0.580813958f,0.812250587f,0.583308653f,
0.810457198f,0.585797857f,0.808656182f,0.588281548f,0.806847554f,0.590759702f,0.805031331f,0.593232295f,0.803207531f,0.595699304f,0.801376172f,0.598160707f,
0.799537269f,0.600616479f,0.797690841f,0.603066599f,0.795836905f,0.605511041f,0.793975478f,0.607949785f,0.792106577f,0.610382806f,0.790230221f,0.612810082f,
0.788346428f,0.615231591f,0.786455214f,0.617647308f,0.784556597f,0.620057212f,0.782650596f,0.622461279f,0.780737229f,0.624859488f,0.778816512f,0.627251815f,
0.776888466f,0.629638239f,0.774953107f,0.632018736f,0.773010453f,0.634393284f,0.771060524f,0.636761861f,0.769103338f,0.639124445f,0.767138912f,0.641481013f,
0.765167266f,0.643831543f,0.763188417f,0.646176013f,0.761202385f,0.648514401f,0.759209189f,0.650846685f,0.757208847f,0.653172843f,0.755201377f,0.655492853f,
0.753186799f,0.657806693f,0.751165132f,0.660114342f,0.749136395f,0.662415778f,0.747100606f,0.664710978f,0.745057785f,0.666999922f,0.743007952f,0.669282588f,
0.740951125f,0.671558955f,0.738887324f,0.673829f,0.736816569f,0.676092704f,0.734738878f,0.678350043f,0.732654272f,0.680600998f,0.730562769f,0.682845546f,
0.72846439f,0.685083668f,0.726359155f,0.687315341f,0.724247083f,0.689540545f,0.722128194f,0.691759258f,0.720002508f,0.693971461f,0.717870045f,0.696177131f,
0.715730825f,0.698376249f,0.713584869f,0.700568794f,0.711432196f,0.702754744f,0.709272826f,0.70493408f,0.707106781f,0.707106781f,0.70493408f,0.709272826f,
0.702754744f,0.711432196f,0.700568794f,0.713584869f,0.698376249f,0.715730825f,0.696177131f,0.717870045f,0.693971461f,0.720002508f,0.691759258f,0.722128194f,
0.689540545f,0.724247083f,0.687315341f,0.726359155f,0.685083668f,0.72846439f,0.682845546f,0.730562769f,0.680600998f,0.732654272f,0.678350043f,0.734738878f,
0.676092704f,0.736816569f,0.673829f,0.738887324f,0.671558955f,0.740951125f,0.669282588f,0.743007952f,0.666999922f,0.745057785f,0.664710978f,0.747100606f,
0.662415778f,0.749136395f,0.660114342f,0.751165132f,0.657806693f,0.753186799f,0.655492853f,0.755201377f,0.653172843f,0.757208847f,0.650846685f,0.759209189f,
0.648514401f,0.761202385f,0.646176013f,0.763188417f,0.643831543f,0.765167266f,0.641481013f,0.767138912f,0.639124445f,0.769103338f,0.636761861f,0.771060524f,
0.634393284f,0.773010453f,0.632018736f,0.774953107f,0.629638239f,0.776888466f,0.627251815f,0.778816512f,0.624859488f,0.780737229f,0.622461279f,0.782650596f,
0.620057212f,0.784556597f,0.617647308f,0.786455214f,0.615231591f,0.788346428f,0.612810082f,0.790230221f,0.610382806f,0.792106577f,0.607949785f,0.793975478f,
0.605511041f,0.795836905f,0.603066599f,0.797690841f,0.600616479f,0.799537269f,0.598160707f,0.801376172f,0.595699304f,0.803207531f,0.593232295f,0.805031331f,
0.590759702f,0.806847554f,0.588281548f,0.808656182f,0.585797857f,0.810457198f,0.583308653f,0.812250587f,0.580813958f,0.81403633f,0.578313796f,0.815814411f,
0.575808191f,0.817584813f,0.573297167f,0.81934752f,0.570780746f,0.821102515f,0.568258953f,0.822849781f,0.565731811f,0.824589303f,0.563199344f,0.826321063f,
0.560661576f,0.828045045f,0.558118531f,0.829761234f,0.555570233f,0.831469612f,0.553016706f,0.833170165f,0.550457973f,0.834862875f,0.547894059f,0.836547727f,
0.545324988f,0.838224706f,0.542750785f,0.839893794f,0.540171473f,0.841554977f,0.537587076f,0.84320824f,0.53499762f,0.844853565f,0.532403128f,0.846490939f,
0.529803625f,0.848120345f,0.527199135f,0.849741768f,0.524589683f,0.851355193f,0.521975293f,0.852960605f,0.51935599f,0.854557988f,0.516731799f,0.856147328f,
0.514102744f,0.85772861f,0.51146885f,0.859301818f,0.508830143f,0.860866939f,0.506186645f,0.862423956f,0.503538384f,0.863972856f,0.500885383f,0.865513624f,
0.498227667f,0.867046246f,0.495565262f,0.868570706f,0.492898192f,0.870086991f,0.490226483f,0.871595087f,0.48755016f,0.873094978f,0.484869248f,0.874586652f,
0.482183772f,0.876070094f,0.479493758f,0.87754529f,0.47679923f,0.879012226f,0.474100215f,0.880470889f,0.471396737f,0.881921264f,0.468688822f,0.883363339f,
0.465976496f,0.884797098f,0.463259784f,0.88622253f,0.460538711f,0.88763962f,0.457813304f,0.889048356f,0.455083587f,0.890448723f,0.452349587f,0.891840709f,
0.44961133f,0.893224301f,0.44686884f,0.894599486f,0.444122145f,0.89596625f,0.441371269f,0.897324581f,0.438616239f,0.898674466f,0.43585708f,0.900015892f,
0.433093819f,0.901348847f,0.430326481f,0.902673318f,0.427555093f,0.903989293f,0.424779681f,0.905296759f,0.422000271f,0.906595705f,0.419216888f,0.907886116f,
0.41642956f,0.909167983f,0.413638312f,0.910441292f,0.410843171f,0.911706032f,0.408044163f,0.91296219f,0.405241314f,0.914209756f,0.402434651f,0.915448716f,
0.3996242f,0.91667906f,0.396809987f,0.917900776f,0.39399204f,0.919113852f,0.391170384f,0.920318277f,0.388345047f,0.921514039f,0.385516054f,0.922701128f,
0.382683432f,0.923879533f,0.379847209f,0.925049241f,0.37700741f,0.926210242f,0.374164063f,0.927362526f,0.371317194f,0.92850608f,0.36846683f,0.929640896f,
0.365612998f,0.930766961f,0.362755724f,0.931884266f,0.359895037f,0.932992799f,0.357030961f,0.93409255f,0.354163525f,0.93518351f,0.351292756f,0.936265667f,
0.34841868f,0.937339012f,0.345541325f,0.938403534f,0.342660717f,0.939459224f,0.339776884f,0.940506071f,0.336889853f,0.941544065f,0.333999651f,0.942573198f,
0.331106306f,0.943593458f,0.328209844f,0.944604837f,0.325310292f,0.945607325f,0.322407679f,0.946600913f,0.319502031f,0.947585591f,0.316593376f,0.94856135f,
0.31368174f,0.949528181f,0.310767153f,0.950486074f,0.30784964f,0.951435021f,0.30492923f,0.952375013f,0.302005949f,0.95330604f,0.299079826f,0.954228095f,
0.296150888f,0.955141168f,0.293219163f,0.956045251f,0.290284677f,0.956940336f,0.28734746f,0.957826413f,0.284407537f,0.958703475f,0.281464938f,0.959571513f,
0.278519689f,0.960430519f,0.275571819f,0.961280486f,0.272621355f,0.962121404f,0.269668326f,0.962953267f,0.266712757f,0.963776066f,0.263754679f,0.964589793f,
0.260794118f,0.965394442f,0.257831102f,0.966190003f,0.25486566f,0.966976471f,0.251897818f,0.967753837f,0.248927606f,0.968522094f,0.24595505f,0.969281235f,
0.24298018f,0.970031253f,0.240003022f,0.970772141f,0.237023606f,0.971503891f,0.234041959f,0.972226497f,0.231058108f,0.972939952f,0.228072083f,0.97364425f,
0.225083911f,0.974339383f,0.222093621f,0.975025345f,0.21910124f,0.97570213f,0.216106797f,0.976369731f,0.21311032f,0.977028143f,0.210111837f,0.977677358f,
0.207111376f,0.978317371f,0.204108966f,0.978948175f,0.201104635f,0.979569766f,0.198098411f,0.980182136f,0.195090322f,0.98078528f,0.192080397f,0.981379193f,
0.189068664f,0.981963869f,0.186055152f,0.982539302f,0.183039888f,0.983105487f,0.180022901f,0.983662419f,0.17700422f,0.984210092f,0.173983873f,0.984748502f,
0.170961889f,0.985277642f,0.167938295f,0.985797509f,0.16491312f,0.986308097f,0.161886394f,0.986809402f,0.158858143f,0.987301418f,0.155828398f,0.987784142f,
0.152797185f,0.988257568f,0.149764535f,0.988721692f,0.146730474f,0.98917651f,0.143695033f,0.989622017f,0.140658239f,0.99005821f,0.137620122f,0.990485084f,
0.134580709f,0.990902635f,0.131540029f,0.99131086f,0.128498111f,0.991709754f,0.125454983f,0.992099313f,0.122410675f,0.992479535f,0.119365215f,0.992850414f,
0.116318631f,0.993211949f,0.113270952f,0.993564136f,0.110222207f,0.99390697f,0.107172425f,0.994240449f,0.104121634f,0.994564571f,0.101069863f,0.994879331f,
0.0980171403f,0.995184727f,0.0949634953f,0.995480755f,0.0919089565f,0.995767414f,0.0888535526f,0.996044701f,0.0857973123f,0.996312612f,0.0827402645f,0.996571146f,
0.079682438f,0.996820299f,0.0766238614f,0.99706007f,0.0735645636f,0.997290457f,0.0705045734f,0.997511456f,0.0674439196f,0.997723067f,0.0643826309f,0.997925286f,
0.0613207363f,0.998118113f,0.0582582645f,0.998301545f,0.0551952443f,0.998475581f,0.0521317047f,0.998640218f,0.0490676743f,0.998795456f,0.0460031821f,0.998941293f,
0.0429382569f,0.999077728f,0.0398729276f,0.999204759f,0.0368072229f,0.999322385f,0.0337411719f,0.999430605f,0.0306748032f,0.999529418f,0.0276081458f,0.999618822f,
0.0245412285f,0.999698819f,0.0214740803f,0.999769405f,0.0184067299f,0.999830582f,0.0153392063f,0.999882347f,0.0122715383f,0.999924702f,0.00920375478f,0.999957645f,
0.00613588465f,0.999981175f,0.00306795676f,0.999995294f,0.f,1.f,-0.00306795676f,0.999995294f,-0.00613588465f,0.999981175f,-0.00920375478f,0.999957645f,
-0.0122715383f,0.999924702f,-0.0153392063f,0.999882347f,-0.0184067299f,0.999830582f,-0.0214740803f,0.999769405f,-0.0245412285f,0.999698819f,-0.0276081458f,0.999618822f,
-0.0306748032f,0.999529418f,-0.0337411719f,0.999430605f,-0.0368072229f,0.999322385f,-0.0398729276f,0.999204759f,-0.0429382569f,0.999077728f,-0.0460031821f,0.998941293f,
-0.0490676743f,0.998795456f,-0.0521317047f,0.998640218f,-0.0551952443f,0.998475581f,-0.0582582645f,0.998301545f,-0.0613207363f,0.998118113f,-0.0643826309f,0.997925286f,
-0.0674439196f,0.997723067f,-0.0705045734f,0.997511456f,-0.0735645636f,0.997290457f,-0.0766238614f,0.99706007f,-0.079682438f,0.996820299f,-0.0827402645f,0.996571146f,
-0.0857973123f,0.996312612f,-0.0888535526f,0.996044701f,-0.0919089565f,0.995767414f,-0.0949634953f,0.995480755f,-0.0980171403f,0.995184727f,-0.101069863f,0.994879331f,
-0.104121634f,0.994564571f,-0.107172425f,0.994240449f,-0.110222207f,0.99390697f,-0.113270952f,0.993564136f,-0.116318631f,0.993211949f,-0.119365215f,0.992850414f,
-0.122410675f,0.992479535f,-0.125454983f,0.992099313f,-0.128498111f,0.991709754f,-0.131540029f,0.99131086f,-0.134580709f,0.990902635f,-0.137620122f,0.990485084f,
-0.140658239f,0.99005821f,-0.143695033f,0.989622017f,-0.146730474f,0.98917651f,-0.149764535f,0.988721692f,-0.152797185f,0.988257568f,-0.155828398f,0.987784142f,
-0.158858143f,0.987301418f,-0.161886394f,0.986809402f,-0.16491312f,0.986308097f,-0.167938295f,0.985797509f,-0.170961889f,0.985277642f,-0.173983873f,0.984748502f,
-0.17700422f,0.984210092f,-0.180022901f,0.983662419f,-0.183039888f,0.983105487f,-0.186055152f,0.982539302f,-0.189068664f,0.981963869f,-0.192080397f,0.981379193f,
-0.195090322f,0.98078528f,-0.198098411f,0.980182136f,-0.201104635f,0.979569766f,-0.204108966f,0.978948175f,-0.207111376f,0.978317371f,-0.210111837f,0.977677358f,
-0.21311032f,0.977028143f,-0.216106797f,0.976369731f,-0.21910124f,0.97570213f,-0.222093621f,0.975025345f,-0.225083911f,0.974339383f,-0.228072083f,0.97364425f,
-0.231058108f,0.972939952f,-0.234041959f,0.972226497f,-0.237023606f,0.971503891f,-0.240003022f,0.970772141f,-0.24298018f,0.970031253f,-0.24595505f,0.969281235f,
-0.248927606f,0.968522094f,-0.251897818f,0.967753837f,-0.25486566f,0.966976471f,-0.257831102f,0.966190003f,-0.260794118f,0.965394442f,-0.263754679f,0.964589793f,
-0.266712757f,0.963776066f,-0.269668326f,0.962953267f,-0.272621355f,0.962121404f,-0.275571819f,0.961280486f,-0.278519689f,0.960430519f,-0.281464938f,0.959571513f,
-0.284407537f,0.958703475f,-0.28734746f,0.957826413f,-0.290284677f,0.956940336f,-0.293219163f,0.956045251f,-0.296150888f,0.955141168f,-0.299079826f,0.954228095f,
-0.302005949f,0.95330604f,-0.30492923f,0.952375013f,-0.30784964f,0.951435021f,-0.310767153f,0.950486074f,-0.31368174f,0.949528181f,-0.316593376f,0.94856135f,
-0.319502031f,0.947585591f,-0.322407679f,0.946600913f,-0.325310292f,0.945607325f,-0.328209844f,0.944604837f,-0.331106306f,0.943593458f,-0.333999651f,0.942573198f,
-0.336889853f,0.941544065f,-0.339776884f,0.940506071f,-0.342660717f,0.939459224f,-0.345541325f,0.938403534f,-0.34841868f,0.937339012f,-0.351292756f,0.936265667f,
-0.354163525f,0.93518351f,-0.357030961f,0.93409255f,-0.359895037f,0.932992799f,-0.362755724f,0.931884266f,-0.365612998f,0.930766961f,-0.36846683f,0.929640896f,
-0.371317194f,0.92850608f,-0.374164063f,0.927362526f,-0.37700741f,0.926210242f,-0.379847209f,0.925049241f,-0.382683432f,0.923879533f,-0.385516054f,0.922701128f,
-0.388345047f,0.921514039f,-0.391170384f,0.920318277f,-0.39399204f,0.919113852f,-0.396809987f,0.917900776f,-0.3996242f,0.91667906f,-0.402434651f,0.915448716f,
-0.405241314f,0.914209756f,-0.408044163f,0.91296219f,-0.410843171f,0.911706032f,-0.413638312f,0.910441292f,-0.41642956f,0.909167983f,-0.419216888f,0.907886116f,
-0.422000271f,0.906595705f,-0.424779681f,0.905296759f,-0.427555093f,0.903989293f,-0.430326481f,0.902673318f,-0.433093819f,0.901348847f,-0.43585708f,0.900015892f,
-0.438616239f,0.898674466f,-0.441371269f,0.897324581f,-0.444122145f,0.89596625f,-0.44686884f,0.894599486f,-0.44961133f,0.893224301f,-0.452349587f,0.891840709f,
-0.455083587f,0.890448723f,-0.457813304f,0.889048356f,-0.460538711f,0.88763962f,-0.463259784f,0.88622253f,-0.465976496f,0.884797098f,-0.468688822f,0.883363339f,
-0.471396737f,0.881921264f,-0.474100215f,0.880470889f,-0.47679923f,0.879012226f,-0.479493758f,0.87754529f,-0.482183772f,0.876070094f,-0.484869248f,0.874586652f,
-0.48755016f,0.873094978f,-0.490226483f,0.871595087f,-0.492898192f,0.870086991f,-0.495565262f,0.868570706f,-0.498227667f,0.867046246f,-0.500885383f,0.865513624f,
-0.503538384f,0.863972856f,-0.506186645f,0.862423956f,-0.508830143f,0.860866939f,-0.51146885f,0.859301818f,-0.514102744f,0.85772861f,-0.516731799f,0.856147328f,
-0.51935599f,0.854557988f,-0.521975293f,0.852960605f,-0.524589683f,0.851355193f,-0.527199135f,0.849741768f,-0.529803625f,0.848120345f,-0.532403128f,0.846490939f,
-0.53499762f,0.844853565f,-0.537587076f,0.84320824f,-0.540171473f,0.841554977f,-0.542750785f,0.839893794f,-0.545324988f,0.838224706f,-0.547894059f,0.836547727f,
-0.550457973f,0.834862875f,-0.553016706f,0.833170165f,-0.555570233f,0.831469612f,-0.558118531f,0.829761234f,-0.560661576f,0.828045045f,-0.563199344f,0.826321063f,
-0.565731811f,0.824589303f,-0.568258953f,0.822849781f,-0.570780746f,0.821102515f,-0.573297167f,0.81934752f,-0.575808191f,0.817584813f,-0.578313796f,0.815814411f,
-0.580813958f,0.81403633f,-0.583308653f,0.812250587f,-0.585797857f,0.810457198f,-0.588281548f,0.808656182f,-0.590759702f,0.806847554f,-0.593232295f,0.805031331f,
-0.595699304f,0.803207531f,-0.598160707f,0.801376172f,-0.600616479f,0.799537269f,-0.603066599f,0.797690841f,-0.605511041f,0.795836905f,-0.607949785f,0.793975478f,
-0.610382806f,0.792106577f,-0.612810082f,0.790230221f,-0.615231591f,0.788346428f,-0.617647308f,0.786455214f,-0.620057212f,0.784556597f,-0.622461279f,0.782650596f,
-0.624859488f,0.780737229f,-0.627251815f,0.778816512f,-0.629638239f,0.776888466f,-0.632018736f,0.774953107f,-0.634393284f,0.773010453f,-0.636761861f,0.771060524f,
-0.639124445f,0.769103338f,-0.641481013f,0.767138912f,-0.643831543f,0.765167266f,-0.646176013f,0.763188417f,-0.648514401f,0.761202385f,-0.650846685f,0.759209189f,
-0.653172843f,0.757208847f,-0.655492853f,0.755201377f,-0.657806693f,0.753186799f,-0.660114342f,0.751165132f,-0.662415778f,0.749136395f,-0.664710978f,0.747100606f,
-0.666999922f,0.745057785f,-0.669282588f,0.743007952f,-0.671558955f,0.740951125f,-0.673829f,0.738887324f,-0.676092704f,0.736816569f,-0.678350043f,0.734738878f,
-0.680600998f,0.732654272f,-0.682845546f,0.730562769f,-0.685083668f,0.72846439f,-0.687315341f,0.726359155f,-0.689540545f,0.724247083f,-0.691759258f,0.722128194f,
-0.693971461f,0.720002508f,-0.696177131f,0.717870045f,-0.698376249f,0.715730825f,-0.700568794f,0.713584869f,-0.702754744f,0.711432196f,-0.70493408f,0.709272826f,
-0.707106781f,0.707106781f,-0.709272826f,0.70493408f,-0.711432196f,0.702754744f,-0.713584869f,0.700568794f,-0.715730825f,0.698376249f,-0.717870045f,0.696177131f,
-0.720002508f,0.693971461f,-0.722128194f,0.691759258f,-0.724247083f,0.689540545f,-0.726359155f,0.687315341f,-0.72846439f,0.685083668f,-0.730562769f,0.682845546f,
-0.732654272f,0.680600998f,-0.734738878f,0.678350043f,-0.736816569f,0.676092704f,-0.738887324f,0.673829f,-0.740951125f,0.671558955f,-0.743007952f,0.669282588f,
-0.745057785f,0.666999922f,-0.747100606f,0.664710978f,-0.749136395f,0.662415778f,-0.751165132f,0.660114342f,-0.753186799f,0.657806693f,-0.755201377f,0.655492853f,
-0.757208847f,0.653172843f,-0.759209189f,0.650846685f,-0.761202385f,0.648514401f,-0.763188417f,0.646176013f,-0.765167266f,0.643831543f,-0.767138912f,0.641481013f,
-0.769103338f,0.639124445f,-0.771060524f,0.636761861f,-0.773010453f,0.634393284f,-0.774953107f,0.632018736f,-0.776888466f,0.629638239f,-0.778816512f,0.627251815f,
-0.780737229f,0.624859488f,-0.782650596f,0.622461279f,-0.784556597f,0.620057212f,-0.786455214f,0.617647308f,-0.788346428f,0.615231591f,-0.790230221f,0.612810082f,
-0.792106577f,0.610382806f,-0.793975478f,0.607949785f,-0.795836905f,0.605511041f,-0.797690841f,0.603066599f,-0.799537269f,0.600616479f,-0.801376172f,0.598160707f,
-0.803207531f,0.595699304f,-0.805031331f,0.593232295f,-0.806847554f,0.590759702f,-0.808656182f,0.588281548f,-0.810457198f,0.585797857f,-0.812250587f,0.583308653f,
-0.81403633f,0.580813958f,-0.815814411f,0.578313796f,-0.817584813f,0.575808191f,-0.81934752f,0.573297167f,-0.821102515f,0.570780746f,-0.822849781f,0.568258953f,
-0.824589303f,0.565731811f,-0.826321063f,0.563199344f,-0.828045045f,0.560661576f,-0.829761234f,0.558118531f,-0.831469612f,0.555570233f,-0.833170165f,0.553016706f,
-0.834862875f,0.550457973f,-0.836547727f,0.547894059f,-0.838224706f,0.545324988f,-0.839893794f,0.542750785f,-0.841554977f,0.540171473f,-0.84320824f,0.537587076f,
-0.844853565f,0.53499762f,-0.846490939f,0.532403128f,-0.848120345f,0.529803625f,-0.849741768f,0.527199135f,-0.851355193f,0.524589683f,-0.852960605f,0.521975293f,
-0.854557988f,0.51935599f,-0.856147328f,0.516731799f,-0.85772861f,0.514102744f,-0.859301818f,0.51146885f,-0.860866939f,0.508830143f,-0.862423956f,0.506186645f,
-0.863972856f,0.503538384f,-0.865513624f,0.500885383f,-0.867046246f,0.498227667f,-0.868570706f,0.495565262f,-0.870086991f,0.492898192f,-0.871595087f,0.490226483f,
-0.873094978f,0.48755016f,-0.874586652f,0.484869248f,-0.876070094f,0.482183772f,-0.87754529f,0.479493758f,-0.879012226f,0.47679923f,-0.880470889f,0.474100215f,
-0.881921264f,0.471396737f,-0.883363339f,0.468688822f,-0.884797098f,0.465976496f,-0.88622253f,0.463259784f,-0.88763962f,0.460538711f,-0.889048356f,0.457813304f,
-0.890448723f,0.455083587f,-0.891840709f,0.452349587f,-0.893224301f,0.44961133f,-0.894599486f,0.44686884f,-0.89596625f,0.444122145f,-0.897324581f,0.441371269f,
-0.898674466f,0.438616239f,-0.900015892f,0.43585708f,-0.901348847f,0.433093819f,-0.902673318f,0.430326481f,-0.903989293f,0.427555093f,-0.905296759f,0.424779681f,
-0.906595705f,0.422000271f,-0.907886116f,0.419216888f,-0.909167983f,0.41642956f,-0.910441292f,0.413638312f,-0.911706032f,0.410843171f,-0.91296219f,0.408044163f,
-0.914209756f,0.405241314f,-0.915448716f,0.402434651f,-0.91667906f,0.3996242f,-0.917900776f,0.396809987f,-0.919113852f,0.39399204f,-0.920318277f,0.391170384f,
-0.921514039f,0.388345047f,-0.922701128f,0.385516054f,-0.923879533f,0.382683432f,-0.925049241f,0.379847209f,-0.926210242f,0.37700741f,-0.927362526f,0.374164063f,
-0.92850608f,0.371317194f,-0.929640896f,0.36846683f,-0.930766961f,0.365612998f,-0.931884266f,0.362755724f,-0.932992799f,0.359895037f,-0.93409255f,0.357030961f,
-0.93518351f,0.354163525f,-0.936265667f,0.351292756f,-0.937339012f,0.34841868f,-0.938403534f,0.345541325f,-0.939459224f,0.342660717f,-0.940506071f,0.339776884f,
-0.941544065f,0.336889853f,-0.942573198f,0.333999651f,-0.943593458f,0.331106306f,-0.944604837f,0.328209844f,-0.945607325f,0.325310292f,-0.946600913f,0.322407679f,
-0.947585591f,0.319502031f,-0.94856135f,0.316593376f,-0.949528181f,0.31368174f,-0.950486074f,0.310767153f,-0.951435021f,0.30784964f,-0.952375013f,0.30492923f,
-0.95330604f,0.302005949f,-0.954228095f,0.299079826f,-0.955141168f,0.296150888f,-0.956045251f,0.293219163f,-0.956940336f,0.290284677f,-0.957826413f,0.28734746f,
-0.958703475f,0.284407537f,-0.959571513f,0.281464938f,-0.960430519f,0.278519689f,-0.961280486f,0.275571819f,-0.962121404f,0.272621355f,-0.962953267f,0.269668326f,
-0.963776066f,0.266712757f,-0.964589793f,0.263754679f,-0.965394442f,0.260794118f,-0.966190003f,0.257831102f,-0.966976471f,0.25486566f,-0.967753837f,0.251897818f,
-0.968522094f,0.248927606f,-0.969281235f,0.24595505f,-0.970031253f,0.24298018f,-0.970772141f,0.240003022f,-0.971503891f,0.237023606f,-0.972226497f,0.234041959f,
-0.972939952f,0.231058108f,-0.97364425f,0.228072083f,-0.974339383f,0.225083911f,-0.975025345f,0.222093621f,-0.97570213f,0.21910124f,-0.976369731f,0.216106797f,
-0.977028143f,0.21311032f,-0.977677358f,0.210111837f,-0.978317371f,0.207111376f,-0.978948175f,0.204108966f,-0.979569766f,0.201104635f,-0.980182136f,0.198098411f,
-0.98078528f,0.195090322f,-0.981379193f,0.192080397f,-0.981963869f,0.189068664f,-0.982539302f,0.186055152f,-0.983105487f,0.183039888f,-0.983662419f,0.180022901f,
-0.984210092f,0.17700422f,-0.984748502f,0.173983873f,-0.985277642f,0.170961889f,-0.985797509f,0.167938295f,-0.986308097f,0.16491312f,-0.986809402f,0.161886394f,
-0.987301418f,0.158858143f,-0.987784142f,0.155828398f,-0.988257568f,0.152797185f,-0.988721692f,0.149764535f,-0.98917651f,0.146730474f,-0.989622017f,0.143695033f,
-0.99005821f,0.140658239f,-0.990485084f,0.137620122f,-0.990902635f,0.134580709f,-0.99131086f,0.131540029f,-0.991709754f,0.128498111f,-0.992099313f,0.125454983f,
-0.992479535f,0.122410675f,-0.992850414f,0.119365215f,-0.993211949f,0.116318631f,-0.993564136f,0.113270952f,-0.99390697f,0.110222207f,-0.994240449f,0.107172425f,
-0.994564571f,0.104121634f,-0.994879331f,0.101069863f,-0.995184727f,0.0980171403f,-0.995480755f,0.0949634953f,-0.995767414f,0.0919089565f,-0.996044701f,0.0888535526f,
-0.996312612f,0.0857973123f,-0.996571146f,0.0827402645f,-0.996820299f,0.079682438f,-0.99706007f,0.0766238614f,-0.997290457f,0.0735645636f,-0.997511456f,0.0705045734f,
-0.997723067f,0.0674439196f,-0.997925286f,0.0643826309f,-0.998118113f,0.0613207363f,-0.998301545f,0.0582582645f,-0.998475581f,0.0551952443f,-0.998640218f,0.0521317047f,
-0.998795456f,0.0490676743f,-0.998941293f,0.0460031821f,-0.999077728f,0.0429382569f,-0.999204759f,0.0398729276f,-0.999322385f,0.0368072229f,-0.999430605f,0.0337411719f,
-0.999529418f,0.0306748032f,-0.999618822f,0.0276081458f,-0.999698819f,0.0245412285f,-0.999769405f,0.0214740803f,-0.999830582f,0.0184067299f,-0.999882347f,0.0153392063f,
-0.999924702f,0.0122715383f,-0.999957645f,0.00920375478f,-0.999981175f,0.00613588465f,-0.999995294f,0.00306795676f,-1.f,0.f,-0.999995294f,-0.00306795676f,
-0.999981175f,-0.00613588465f,-0.999957645f,-0.00920375478f,-0.999924702f,-0.0122715383f,-0.999882347f,-0.0153392063f,-0.999830582f,-0.0184067299f,-0.999769405f,-0.0214740803f,
-0.999698819f,-0.0245412285f,-0.999618822f,-0.0276081458f,-0.999529418f,-0.0306748032f,-0.999430605f,-0.0337411719f,-0.999322385f,-0.0368072229f,-0.999204759f,-0.0398729276f,
-0.999077728f,-0.0429382569f,-0.998941293f,-0.0460031821f,-0.998795456f,-0.0490676743f,-0.998640218f,-0.0521317047f,-0.998475581f,-0.0551952443f,-0.998301545f,-0.0582582645f,
-0.998118113f,-0.0613207363f,-0.997925286f,-0.0643826309f,-0.997723067f,-0.0674439196f,-0.997511456f,-0.0705045734f,-0.997290457f,-0.0735645636f,-0.99706007f,-0.0766238614f,
-0.996820299f,-0.079682438f,-0.996571146f,-0.0827402645f,-0.996312612f,-0.0857973123f,-0.996044701f,-0.0888535526f,-0.995767414f,-0.0919089565f,-0.995480755f,-0.0949634953f,
-0.995184727f,-0.0980171403f,-0.994879331f,-0.101069863f,-0.994564571f,-0.104121634f,-0.994240449f,-0.107172425f,-0.99390697f,-0.110222207f,-0.993564136f,-0.113270952f,
-0.993211949f,-0.116318631f,-0.992850414f,-0.119365215f,-0.992479535f,-0.122410675f,-0.992099313f,-0.125454983f,-0.991709754f,-0.128498111f,-0.99131086f,-0.131540029f,
-0.990902635f,-0.134580709f,-0.990485084f,-0.137620122f,-0.99005821f,-0.140658239f,-0.989622017f,-0.143695033f,-0.98917651f,-0.146730474f,-0.988721692f,-0.149764535f,
-0.988257568f,-0.152797185f,-0.987784142f,-0.155828398f,-0.987301418f,-0.158858143f,-0.986809402f,-0.161886394f,-0.986308097f,-0.16491312f,-0.985797509f,-0.167938295f,
-0.985277642f,-0.170961889f,-0.984748502f,-0.173983873f,-0.984210092f,-0.17700422f,-0.983662419f,-0.180022901f,-0.983105487f,-0.183039888f,-0.982539302f,-0.186055152f,
-0.981963869f,-0.189068664f,-0.981379193f,-0.192080397f,-0.98078528f,-0.195090322f,-0.980182136f,-0.198098411f,-0.979569766f,-0.201104635f,-0.978948175f,-0.204108966f,
-0.978317371f,-0.207111376f,-0.977677358f,-0.210111837f,-0.977028143f,-0.21311032f,-0.976369731f,-0.216106797f,-0.97570213f,-0.21910124f,-0.975025345f,-0.222093621f,
-0.974339383f,-0.225083911f,-0.97364425f,-0.228072083f,-0.972939952f,-0.231058108f,-0.972226497f,-0.234041959f,-0.971503891f,-0.237023606f,-0.970772141f,-0.240003022f,
-0.970031253f,-0.24298018f,-0.969281235f,-0.24595505f,-0.968522094f,-0.248927606f,-0.967753837f,-0.251897818f,-0.966976471f,-0.25486566f,-0.966190003f,-0.257831102f,
-0.965394442f,-0.260794118f,-0.964589793f,-0.263754679f,-0.963776066f,-0.266712757f,-0.962953267f,-0.269668326f,-0.962121404f,-0.272621355f,-0.961280486f,-0.275571819f,
-0.960430519f,-0.278519689f,-0.959571513f,-0.281464938f,-0.958703475f,-0.284407537f,-0.957826413f,-0.28734746f,-0.956940336f,-0.290284677f,-0.956045251f,-0.293219163f,
-0.955141168f,-0.296150888f,-0.954228095f,-0.299079826f,-0.95330604f,-0.302005949f,-0.952375013f,-0.30492923f,-0.951435021f,-0.30784964f,-0.950486074f,-0.310767153f,
-0.949528181f,-0.31368174f,-0.94856135f,-0.316593376f,-0.947585591f,-0.319502031f,-0.946600913f,-0.322407679f,-0.945607325f,-0.325310292f,-0.944604837f,-0.328209844f,
-0.943593458f,-0.331106306f,-0.942573198f,-0.333999651f,-0.941544065f,-0.336889853f,-0.940506071f,-0.339776884f,-0.939459224f,-0.342660717f,-0.938403534f,-0.345541325f,
-0.937339012f,-0.34841868f,-0.936265667f,-0.351292756f,-0.93518351f,-0.354163525f,-0.93409255f,-0.357030961f,-0.932992799f,-0.359895037f,-0.931884266f,-0.362755724f,
-0.930766961f,-0.365612998f,-0.929640896f,-0.36846683f,-0.92850608f,-0.371317194f,-0.927362526f,-0.374164063f,-0.926210242f,-0.37700741f,-0.925049241f,-0.379847209f,
-0.923879533f,-0.382683432f,-0.922701128f,-0.385516054f,-0.921514039f,-0.388345047f,-0.920318277f,-0.391170384f,-0.919113852f,-0.39399204f,-0.917900776f,-0.396809987f,
-0.91667906f,-0.3996242f,-0.915448716f,-0.402434651f,-0.914209756f,-0.405241314f,-0.91296219f,-0.408044163f,-0.911706032f,-0.410843171f,-0.910441292f,-0.413638312f,
-0.909167983f,-0.41642956f,-0.907886116f,-0.419216888f,-0.906595705f,-0.422000271f,-0.905296759f,-0.424779681f,-0.903989293f,-0.427555093f,-0.902673318f,-0.430326481f,
-0.901348847f,-0.433093819f,-0.900015892f,-0.43585708f,-0.898674466f,-0.438616239f,-0.897324581f,-0.441371269f,-0.89596625f,-0.444122145f,-0.894599486f,-0.44686884f,
-0.893224301f,-0.44961133f,-0.891840709f,-0.452349587f,-0.890448723f,-0.455083587f,-0.889048356f,-0.457813304f,-0.88763962f,-0.460538711f,-0.88622253f,-0.463259784f,
-0.884797098f,-0.465976496f,-0.883363339f,-0.468688822f,-0.881921264f,-0.471396737f,-0.880470889f,-0.474100215f,-0.879012226f,-0.47679923f,-0.87754529f,-0.479493758f,
-0.876070094f,-0.482183772f,-0.874586652f,-0.484869248f,-0.873094978f,-0.48755016f,-0.871595087f,-0.490226483f,-0.870086991f,-0.492898192f,-0.868570706f,-0.495565262f,
-0.867046246f,-0.498227667f,-0.865513624f,-0.500885383f,-0.863972856f,-0.503538384f,-0.862423956f,-0.506186645f,-0.860866939f,-0.508830143f,-0.859301818f,-0.51146885f,
-0.85772861f,-0.514102744f,-0.856147328f,-0.516731799f,-0.854557988f,-0.51935599f,-0.852960605f,-0.521975293f,-0.851355193f,-0.524589683f,-0.849741768f,-0.527199135f,
-0.848120345f,-0.529803625f,-0.846490939f,-0.532403128f,-0.844853565f,-0.53499762f,-0.84320824f,-0.537587076f,-0.841554977f,-0.540171473f,-0.839893794f,-0.542750785f,
-0.838224706f,-0.545324988f,-0.836547727f,-0.547894059f,-0.834862875f,-0.550457973f,-0.833170165f,-0.553016706f,-0.831469612f,-0.555570233f,-0.829761234f,-0.558118531f,
-0.828045045f,-0.560661576f,-0.826321063f,-0.563199344f,-0.824589303f,-0.565731811f,-0.822849781f,-0.568258953f,-0.821102515f,-0.570780746f,-0.81934752f,-0.573297167f,
-0.817584813f,-0.575808191f,-0.815814411f,-0.578313796f,-0.81403633f,-0.580813958f,-0.812250587f,-0.583308653f,-0.810457198f,-0.585797857f,-0.808656182f,-0.588281548f,
-0.806847554f,-0.590759702f,-0.805031331f,-0.593232295f,-0.803207531f,-0.595699304f,-0.801376172f,-0.598160707f,-0.799537269f,-0.600616479f,-0.797690841f,-0.603066599f,
-0.795836905f,-0.605511041f,-0.793975478f,-0.607949785f,-0.792106577f,-0.610382806f,-0.790230221f,-0.612810082f,-0.788346428f,-0.615231591f,-0.786455214f,-0.617647308f,
-0.784556597f,-0.620057212f,-0.782650596f,-0.622461279f,-0.780737229f,-0.624859488f,-0.778816512f,-0.627251815f,-0.776888466f,-0.629638239f,-0.774953107f,-0.632018736f,
-0.773010453f,-0.634393284f,-0.771060524f,-0.636761861f,-0.769103338f,-0.639124445f,-0.767138912f,-0.641481013f,-0.765167266f,-0.643831543f,-0.763188417f,-0.646176013f,
-0.761202385f,-0.648514401f,-0.759209189f,-0.650846685f,-0.757208847f,-0.653172843f,-0.755201377f,-0.655492853f,-0.753186799f,-0.657806693f,-0.751165132f,-0.660114342f,
-0.749136395f,-0.662415778f,-0.747100606f,-0.664710978f,-0.745057785f,-0.666999922f,-0.743007952f,-0.669282588f,-0.740951125f,-0.671558955f,-0.738887324f,-0.673829f,
-0.736816569f,-0.676092704f,-0.734738878f,-0.678350043f,-0.732654272f,-0.680600998f,-0.730562769f,-0.682845546f,-0.72846439f,-0.685083668f,-0.726359155f,-0.687315341f,
-0.724247083f,-0.689540545f,-0.722128194f,-0.691759258f,-0.720002508f,-0.693971461f,-0.717870045f,-0.696177131f,-0.715730825f,-0.698376249f,-0.713584869f,-0.700568794f,
-0.711432196f,-0.702754744f,-0.709272826f,-0.70493408f,-0.707106781f,-0.707106781f,-0.70493408f,-0.709272826f,-0.702754744f,-0.711432196f,-0.700568794f,-0.713584869f,
-0.698376249f,-0.715730825f,-0.696177131f,-0.717870045f,-0.693971461f,-0.720002508f,-0.691759258f,-0.722128194f,-0.689540545f,-0.724247083f,-0.687315341f,-0.726359155f,
-0.685083668f,-0.72846439f,-0.682845546f,-0.730562769f,-0.680600998f,-0.732654272f,-0.678350043f,-0.734738878f,-0.676092704f,-0.736816569f,-0.673829f,-0.738887324f,
-0.671558955f,-0.740951125f,-0.669282588f,-0.743007952f,-0.666999922f,-0.745057785f,-0.664710978f,-0.747100606f,-0.662415778f,-0.749136395f,-0.660114342f,-0.751165132f,
-0.657806693f,-0.753186799f,-0.655492853f,-0.755201377f,-0.653172843f,-0.757208847f,-0.650846685f,-0.759209189f,-0.648514401f,-0.761202385f,-0.646176013f,-0.763188417f,
-0.643831543f,-0.765167266f,-0.641481013f,-0.767138912f,-0.639124445f,-0.769103338f,-0.636761861f,-0.771060524f,-0.634393284f,-0.773010453f,-0.632018736f,-0.774953107f,
-0.629638239f,-0.776888466f,-0.627251815f,-0.778816512f,-0.624859488f,-0.780737229f,-0.622461279f,-0.782650596f,-0.620057212f,-0.784556597f,-0.617647308f,-0.786455214f,
-0.615231591f,-0.788346428f,-0.612810082f,-0.790230221f,-0.610382806f,-0.792106577f,-0.607949785f,-0.793975478f,-0.605511041f,-0.795836905f,-0.603066599f,-0.797690841f,
-0.600616479f,-0.799537269f,-0.598160707f,-0.801376172f,-0.595699304f,-0.803207531f,-0.593232295f,-0.805031331f,-0.590759702f,-0.806847554f,-0.588281548f,-0.808656182f,
-0.585797857f,-0.810457198f,-0.583308653f,-0.812250587f,-0.580813958f,-0.81403633f,-0.578313796f,-0.815814411f,-0.575808191f,-0.817584813f,-0.573297167f,-0.81934752f,
-0.570780746f,-0.821102515f,-0.568258953f,-0.822849781f,-0.565731811f,-0.824589303f,-0.563199344f,-0.826321063f,-0.560661576f,-0.828045045f,-0.558118531f,-0.829761234f,
-0.555570233f,-0.831469612f,-0.553016706f,-0.833170165f,-0.550457973f,-0.834862875f,-0.547894059f,-0.836547727f,-0.545324988f,-0.838224706f,-0.542750785f,-0.839893794f,
-0.540171473f,-0.841554977f,-0.537587076f,-0.84320824f,-0.53499762f,-0.844853565f,-0.532403128f,-0.846490939f,-0.529803625f,-0.848120345f,-0.527199135f,-0.849741768f,
-0.524589683f,-0.851355193f,-0.521975293f,-0.852960605f,-0.51935599f,-0.854557988f,-0.516731799f,-0.856147328f,-0.514102744f,-0.85772861f,-0.51146885f,-0.859301818f,
-0.508830143f,-0.860866939f,-0.506186645f,-0.862423956f,-0.503538384f,-0.863972856f,-0.500885383f,-0.865513624f,-0.498227667f,-0.867046246f,-0.495565262f,-0.868570706f,
-0.492898192f,-0.870086991f,-0.490226483f,-0.871595087f,-0.48755016f,-0.873094978f,-0.484869248f,-0.874586652f,-0.482183772f,-0.876070094f,-0.479493758f,-0.87754529f,
-0.47679923f,-0.879012226f,-0.474100215f,-0.880470889f,-0.471396737f,-0.881921264f,-0.468688822f,-0.883363339f,-0.465976496f,-0.884797098f,-0.463259784f,-0.88622253f,
-0.460538711f,-0.88763962f,-0.457813304f,-0.889048356f,-0.455083587f,-0.890448723f,-0.452349587f,-0.891840709f,-0.44961133f,-0.893224301f,-0.44686884f,-0.894599486f,
-0.444122145f,-0.89596625f,-0.441371269f,-0.897324581f,-0.438616239f,-0.898674466f,-0.43585708f,-0.900015892f,-0.433093819f,-0.901348847f,-0.430326481f,-0.902673318f,
-0.427555093f,-0.903989293f,-0.424779681f,-0.905296759f,-0.422000271f,-0.906595705f,-0.419216888f,-0.907886116f,-0.41642956f,-0.909167983f,-0.413638312f,-0.910441292f,
-0.410843171f,-0.911706032f,-0.408044163f,-0.91296219f,-0.405241314f,-0.914209756f,-0.402434651f,-0.915448716f,-0.3996242f,-0.91667906f,-0.396809987f,-0.917900776f,
-0.39399204f,-0.919113852f,-0.391170384f,-0.920318277f,-0.388345047f,-0.921514039f,-0.385516054f,-0.922701128f,-0.382683432f,-0.923879533f,-0.379847209f,-0.925049241f,
-0.37700741f,-0.926210242f,-0.374164063f,-0.927362526f,-0.371317194f,-0.92850608f,-0.36846683f,-0.929640896f,-0.365612998f,-0.930766961f,-0.362755724f,-0.931884266f,
-0.359895037f,-0.932992799f,-0.357030961f,-0.93409255f,-0.354163525f,-0.93518351f,-0.351292756f,-0.936265667f,-0.34841868f,-0.937339012f,-0.345541325f,-0.938403534f,
-0.342660717f,-0.939459224f,-0.339776884f,-0.940506071f,-0.336889853f,-0.941544065f,-0.333999651f,-0.942573198f,-0.331106306f,-0.943593458f,-0.328209844f,-0.944604837f,
-0.325310292f,-0.945607325f,-0.322407679f,-0.946600913f,-0.319502031f,-0.947585591f,-0.316593376f,-0.94856135f,-0.31368174f,-0.949528181f,-0.310767153f,-0.950486074f,
-0.30784964f,-0.951435021f,-0.30492923f,-0.952375013f,-0.302005949f,-0.95330604f,-0.299079826f,-0.954228095f,-0.296150888f,-0.955141168f,-0.293219163f,-0.956045251f,
-0.290284677f,-0.956940336f,-0.28734746f,-0.957826413f,-0.284407537f,-0.958703475f,-0.281464938f,-0.959571513f,-0.278519689f,-0.960430519f,-0.275571819f,-0.961280486f,
-0.272621355f,-0.962121404f,-0.269668326f,-0.962953267f,-0.266712757f,-0.963776066f,-0.263754679f,-0.964589793f,-0.260794118f,-0.965394442f,-0.257831102f,-0.966190003f,
-0.25486566f,-0.966976471f,-0.251897818f,-0.967753837f,-0.248927606f,-0.968522094f,-0.24595505f,-0.969281235f,-0.24298018f,-0.970031253f,-0.240003022f,-0.970772141f,
-0.237023606f,-0.971503891f,-0.234041959f,-0.972226497f,-0.231058108f,-0.972939952f,-0.228072083f,-0.97364425f,-0.225083911f,-0.974339383f,-0.222093621f,-0.975025345f,
-0.21910124f,-0.97570213f,-0.216106797f,-0.976369731f,-0.21311032f,-0.977028143f,-0.210111837f,-0.977677358f,-0.207111376f,-0.978317371f,-0.204108966f,-0.978948175f,
-0.201104635f,-0.979569766f,-0.198098411f,-0.980182136f,-0.195090322f,-0.98078528f,-0.192080397f,-0.981379193f,-0.189068664f,-0.981963869f,-0.186055152f,-0.982539302f,
-0.183039888f,-0.983105487f,-0.180022901f,-0.983662419f,-0.17700422f,-0.984210092f,-0.173983873f,-0.984748502f,-0.170961889f,-0.985277642f,-0.167938295f,-0.985797509f,
-0.16491312f,-0.986308097f,-0.161886394f,-0.986809402f,-0.158858143f,-0.987301418f,-0.155828398f,-0.987784142f,-0.152797185f,-0.988257568f,-0.149764535f,-0.988721692f,
-0.146730474f,-0.98917651f,-0.143695033f,-0.989622017f,-0.140658239f,-0.99005821f,-0.137620122f,-0.990485084f,-0.134580709f,-0.990902635f,-0.131540029f,-0.99131086f,
-0.128498111f,-0.991709754f,-0.125454983f,-0.992099313f,-0.122410675f,-0.992479535f,-0.119365215f,-0.992850414f,-0.116318631f,-0.993211949f,-0.113270952f,-0.993564136f,
-0.110222207f,-0.99390697f,-0.107172425f,-0.994240449f,-0.104121634f,-0.994564571f,-0.101069863f,-0.994879331f,-0.0980171403f,-0.995184727f,-0.0949634953f,-0.995480755f,
-0.0919089565f,-0.995767414f,-0.0888535526f,-0.996044701f,-0.0857973123f,-0.996312612f,-0.0827402645f,-0.996571146f,-0.079682438f,-0.996820299f,-0.0766238614f,-0.99706007f,
-0.0735645636f,-0.997290457f,-0.0705045734f,-0.997511456f,-0.0674439196f,-0.997723067f,-0.0643826309f,-0.997925286f,-0.0613207363f,-0.998118113f,-0.0582582645f,-0.998301545f,
-0.0551952443f,-0.998475581f,-0.0521317047f,-0.998640218f,-0.0490676743f,-0.998795456f,-0.0460031821f,-0.998941293f,-0.0429382569f,-0.999077728f,-0.0398729276f,-0.999204759f,
-0.0368072229f,-0.999322385f,-0.0337411719f,-0.999430605f,-0.0306748032f,-0.999529418f,-0.0276081458f,-0.999618822f,-0.0245412285f,-0.999698819f,-0.0214740803f,-0.999769405f,
-0.0184067299f,-0.999830582f,-0.0153392063f,-0.999882347f,-0.0122715383f,-0.999924702f,-0.00920375478f,-0.999957645f,-0.00613588465f,-0.999981175f,-0.00306795676f,-0.999995294f,
0.f,-1.f,0.00306795676f,-0.999995294f,0.00613588465f,-0.999981175f,0.00920375478f,-0.999957645f,0.0122715383f,-0.999924702f,0.0153392063f,-0.999882347f,
0.0184067299f,-0.999830582f,0.0214740803f,-0.999769405f,0.0245412285f,-0.999698819f,0.0276081458f,-0.999618822f,0.0306748032f,-0.999529418f,0.0337411719f,-0.999430605f,
0.0368072229f,-0.999322385f,0.0398729276f,-0.999204759f,0.0429382569f,-0.999077728f,0.0460031821f,-0.998941293f,0.0490676743f,-0.998795456f,0.0521317047f,-0.998640218f,
0.0551952443f,-0.998475581f,0.0582582645f,-0.998301545f,0.0613207363f,-0.998118113f,0.0643826309f,-0.997925286f,0.0674439196f,-0.997723067f,0.0705045734f,-0.997511456f,
0.0735645636f,-0.997290457f,0.0766238614f,-0.99706007f,0.079682438f,-0.996820299f,0.0827402645f,-0.996571146f,0.0857973123f,-0.996312612f,0.0888535526f,-0.996044701f,
0.0919089565f,-0.995767414f,0.0949634953f,-0.995480755f,0.0980171403f,-0.995184727f,0.101069863f,-0.994879331f,0.104121634f,-0.994564571f,0.107172425f,-0.994240449f,
0.110222207f,-0.99390697f,0.113270952f,-0.993564136f,0.116318631f,-0.993211949f,0.119365215f,-0.992850414f,0.122410675f,-0.992479535f,0.125454983f,-0.992099313f,
0.128498111f,-0.991709754f,0.131540029f,-0.99131086f,0.134580709f,-0.990902635f,0.137620122f,-0.990485084f,0.140658239f,-0.99005821f,0.143695033f,-0.989622017f,
0.146730474f,-0.98917651f,0.149764535f,-0.988721692f,0.152797185f,-0.988257568f,0.155828398f,-0.987784142f,0.158858143f,-0.987301418f,0.161886394f,-0.986809402f,
0.16491312f,-0.986308097f,0.167938295f,-0.985797509f,0.170961889f,-0.985277642f,0.173983873f,-0.984748502f,0.17700422f,-0.984210092f,0.180022901f,-0.983662419f,
0.183039888f,-0.983105487f,0.186055152f,-0.982539302f,0.189068664f,-0.981963869f,0.192080397f,-0.981379193f,0.195090322f,-0.98078528f,0.198098411f,-0.980182136f,
0.201104635f,-0.979569766f,0.204108966f,-0.978948175f,0.207111376f,-0.978317371f,0.210111837f,-0.977677358f,0.21311032f,-0.977028143f,0.216106797f,-0.976369731f,
0.21910124f,-0.97570213f,0.222093621f,-0.975025345f,0.225083911f,-0.974339383f,0.228072083f,-0.97364425f,0.231058108f,-0.972939952f,0.234041959f,-0.972226497f,
0.237023606f,-0.971503891f,0.240003022f,-0.970772141f,0.24298018f,-0.970031253f,0.24595505f,-0.969281235f,0.248927606f,-0.968522094f,0.251897818f,-0.967753837f,
0.25486566f,-0.966976471f,0.257831102f,-0.966190003f,0.260794118f,-0.965394442f,0.263754679f,-0.964589793f,0.266712757f,-0.963776066f,0.269668326f,-0.962953267f,
0.272621355f,-0.962121404f,0.275571819f,-0.961280486f,0.278519689f,-0.960430519f,0.281464938f,-0.959571513f,0.284407537f,-0.958703475f,0.28734746f,-0.957826413f,
0.290284677f,-0.956940336f,0.293219163f,-0.956045251f,0.296150888f,-0.955141168f,0.299079826f,-0.954228095f,0.302005949f,-0.95330604f,0.30492923f,-0.952375013f,
0.30784964f,-0.951435021f,0.310767153f,-0.950486074f,0.31368174f,-0.949528181f,0.316593376f,-0.94856135f,0.319502031f,-0.947585591f,0.322407679f,-0.946600913f,
0.325310292f,-0.945607325f,0.328209844f,-0.944604837f,0.331106306f,-0.943593458f,0.333999651f,-0.942573198f,0.336889853f,-0.941544065f,0.339776884f,-0.940506071f,
0.342660717f,-0.939459224f,0.345541325f,-0.938403534f,0.34841868f,-0.937339012f,0.351292756f,-0.936265667f,0.354163525f,-0.93518351f,0.357030961f,-0.93409255f,
0.359895037f,-0.932992799f,0.362755724f,-0.931884266f,0.365612998f,-0.930766961f,0.36846683f,-0.929640896f,0.371317194f,-0.92850608f,0.374164063f,-0.927362526f,
0.37700741f,-0.926210242f,0.379847209f,-0.925049241f,0.382683432f,-0.923879533f,0.385516054f,-0.922701128f,0.388345047f,-0.921514039f,0.391170384f,-0.920318277f,
0.39399204f,-0.919113852f,0.396809987f,-0.917900776f,0.3996242f,-0.91667906f,0.402434651f,-0.915448716f,0.405241314f,-0.914209756f,0.408044163f,-0.91296219f,
0.410843171f,-0.911706032f,0.413638312f,-0.910441292f,0.41642956f,-0.909167983f,0.419216888f,-0.907886116f,0.422000271f,-0.906595705f,0.424779681f,-0.905296759f,
0.427555093f,-0.903989293f,0.430326481f,-0.902673318f,0.433093819f,-0.901348847f,0.43585708f,-0.900015892f,0.438616239f,-0.898674466f,0.441371269f,-0.897324581f,
0.444122145f,-0.89596625f,0.44686884f,-0.894599486f,0.44961133f,-0.893224301f,0.452349587f,-0.891840709f,0.455083587f,-0.890448723f,0.457813304f,-0.889048356f,
0.460538711f,-0.88763962f,0.463259784f,-0.88622253f,0.465976496f,-0.884797098f,0.468688822f,-0.883363339f,0.471396737f,-0.881921264f,0.474100215f,-0.880470889f,
0.47679923f,-0.879012226f,0.479493758f,-0.87754529f,0.482183772f,-0.876070094f,0.484869248f,-0.874586652f,0.48755016f,-0.873094978f,0.490226483f,-0.871595087f,
0.492898192f,-0.870086991f,0.495565262f,-0.868570706f,0.498227667f,-0.867046246f,0.500885383f,-0.865513624f,0.503538384f,-0.863972856f,0.506186645f,-0.862423956f,
0.508830143f,-0.860866939f,0.51146885f,-0.859301818f,0.514102744f,-0.85772861f,0.516731799f,-0.856147328f,0.51935599f,-0.854557988f,0.521975293f,-0.852960605f,
0.524589683f,-0.851355193f,0.527199135f,-0.849741768f,0.529803625f,-0.848120345f,0.532403128f,-0.846490939f,0.53499762f,-0.844853565f,0.537587076f,-0.84320824f,
0.540171473f,-0.841554977f,0.542750785f,-0.839893794f,0.545324988f,-0.838224706f,0.547894059f,-0.836547727f,0.550457973f,-0.834862875f,0.553016706f,-0.833170165f,
0.555570233f,-0.831469612f,0.558118531f,-0.829761234f,0.560661576f,-0.828045045f,0.563199344f,-0.826321063f,0.565731811f,-0.824589303f,0.568258953f,-0.822849781f,
0.570780746f,-0.821102515f,0.573297167f,-0.81934752f,0.575808191f,-0.817584813f,0.578313796f,-0.815814411f,0.580813958f,-0.81403633f,0.583308653f,-0.812250587f,
0.585797857f,-0.810457198f,0.588281548f,-0.808656182f,0.590759702f,-0.806847554f,0.593232295f,-0.805031331f,0.595699304f,-0.803207531f,0.598160707f,-0.801376172f,
0.600616479f,-0.799537269f,0.603066599f,-0.797690841f,0.605511041f,-0.795836905f,0.607949785f,-0.793975478f,0.610382806f,-0.792106577f,0.612810082f,-0.790230221f,
0.615231591f,-0.788346428f,0.617647308f,-0.786455214f,0.620057212f,-0.784556597f,0.622461279f,-0.782650596f,0.624859488f,-0.780737229f,0.627251815f,-0.778816512f,
0.629638239f,-0.776888466f,0.632018736f,-0.774953107f,0.634393284f,-0.773010453f,0.636761861f,-0.771060524f,0.639124445f,-0.769103338f,0.641481013f,-0.767138912f,
0.643831543f,-0.765167266f,0.646176013f,-0.763188417f,0.648514401f,-0.761202385f,0.650846685f,-0.759209189f,0.653172843f,-0.757208847f,0.655492853f,-0.755201377f,
0.657806693f,-0.753186799f,0.660114342f,-0.751165132f,0.662415778f,-0.749136395f,0.664710978f,-0.747100606f,0.666999922f,-0.745057785f,0.669282588f,-0.743007952f,
0.671558955f,-0.740951125f,0.673829f,-0.738887324f,0.676092704f,-0.736816569f,0.678350043f,-0.734738878f,0.680600998f,-0.732654272f,0.682845546f,-0.730562769f,
0.685083668f,-0.72846439f,0.687315341f,-0.726359155f,0.689540545f,-0.724247083f,0.691759258f,-0.722128194f,0.693971461f,-0.720002508f,0.696177131f,-0.717870045f,
0.698376249f,-0.715730825f,0.700568794f,-0.713584869f,0.702754744f,-0.711432196f,0.70493408f,-0.709272826f,0.707106781f,-0.707106781f,0.709272826f,-0.70493408f,
0.711432196f,-0.702754744f,0.713584869f,-0.700568794f,0.715730825f,-0.698376249f,0.717870045f,-0.696177131f,0.720002508f,-0.693971461f,0.722128194f,-0.691759258f,
0.724247083f,-0.689540545f,0.726359155f,-0.687315341f,0.72846439f,-0.685083668f,0.730562769f,-0.682845546f,0.732654272f,-0.680600998f,0.734738878f,-0.678350043f,
0.736816569f,-0.676092704f,0.738887324f,-0.673829f,0.740951125f,-0.671558955f,0.743007952f,-0.669282588f,0.745057785f,-0.666999922f,0.747100606f,-0.664710978f,
0.749136395f,-0.662415778f,0.751165132f,-0.660114342f,0.753186799f,-0.657806693f,0.755201377f,-0.655492853f,0.757208847f,-0.653172843f,0.759209189f,-0.650846685f,
0.761202385f,-0.648514401f,0.763188417f,-0.646176013f,0.765167266f,-0.643831543f,0.767138912f,-0.641481013f,0.769103338f,-0.639124445f,0.771060524f,-0.636761861f,
0.773010453f,-0.634393284f,0.774953107f,-0.632018736f,0.776888466f,-0.629638239f,0.778816512f,-0.627251815f,0.780737229f,-0.624859488f,0.782650596f,-0.622461279f,
0.784556597f,-0.620057212f,0.786455214f,-0.617647308f,0.788346428f,-0.615231591f,0.790230221f,-0.612810082f,0.792106577f,-0.610382806f,0.793975478f,-0.607949785f,
0.795836905f,-0.605511041f,0.797690841f,-0.603066599f,0.799537269f,-0.600616479f,0.801376172f,-0.598160707f,0.803207531f,-0.595699304f,0.805031331f,-0.593232295f,
0.806847554f,-0.590759702f,0.808656182f,-0.588281548f,0.810457198f,-0.585797857f,0.812250587f,-0.583308653f,0.81403633f,-0.580813958f,0.815814411f,-0.578313796f,
0.817584813f,-0.575808191f,0.81934752f,-0.573297167f,0.821102515f,-0.570780746f,0.822849781f,-0.568258953f,0.824589303f,-0.565731811f,0.826321063f,-0.563199344f,
0.828045045f,-0.560661576f,0.829761234f,-0.558118531f,0.831469612f,-0.555570233f,0.833170165f,-0.553016706f,0.834862875f,-0.550457973f,0.836547727f,-0.547894059f,
0.838224706f,-0.545324988f,0.839893794f,-0.542750785f,0.841554977f,-0.540171473f,0.84320824f,-0.537587076f,0.844853565f,-0.53499762f,0.846490939f,-0.532403128f,
0.848120345f,-0.529803625f,0.849741768f,-0.527199135f,0.851355193f,-0.524589683f,0.852960605f,-0.521975293f,0.854557988f,-0.51935599f,0.856147328f,-0.516731799f,
0.85772861f,-0.514102744f,0.859301818f,-0.51146885f,0.860866939f,-0.508830143f,0.862423956f,-0.506186645f,0.863972856f,-0.503538384f,0.865513624f,-0.500885383f,
0.867046246f,-0.498227667f,0.868570706f,-0.495565262f,0.870086991f,-0.492898192f,0.871595087f,-0.490226483f,0.873094978f,-0.48755016f,0.874586652f,-0.484869248f,
0.876070094f,-0.482183772f,0.87754529f,-0.479493758f,0.879012226f,-0.47679923f,0.880470889f,-0.474100215f,0.881921264f,-0.471396737f,0.883363339f,-0.468688822f,
0.884797098f,-0.465976496f,0.88622253f,-0.463259784f,0.88763962f,-0.460538711f,0.889048356f,-0.457813304f,0.890448723f,-0.455083587f,0.891840709f,-0.452349587f,
0.893224301f,-0.44961133f,0.894599486f,-0.44686884f,0.89596625f,-0.444122145f,0.897324581f,-0.441371269f,0.898674466f,-0.438616239f,0.900015892f,-0.43585708f,
0.901348847f,-0.433093819f,0.902673318f,-0.430326481f,0.903989293f,-0.427555093f,0.905296759f,-0.424779681f,0.906595705f,-0.422000271f,0.907886116f,-0.419216888f,
0.909167983f,-0.41642956f,0.910441292f,-0.413638312f,0.911706032f,-0.410843171f,0.91296219f,-0.408044163f,0.914209756f,-0.405241314f,0.915448716f,-0.402434651f,
0.91667906f,-0.3996242f,0.917900776f,-0.396809987f,0.919113852f,-0.39399204f,0.920318277f,-0.391170384f,0.921514039f,-0.388345047f,0.922701128f,-0.385516054f,
0.923879533f,-0.382683432f,0.925049241f,-0.379847209f,0.926210242f,-0.37700741f,0.927362526f,-0.374164063f,0.92850608f,-0.371317194f,0.929640896f,-0.36846683f,
0.930766961f,-0.365612998f,0.931884266f,-0.362755724f,0.932992799f,-0.359895037f,0.93409255f,-0.357030961f,0.93518351f,-0.354163525f,0.936265667f,-0.351292756f,
0.937339012f,-0.34841868f,0.938403534f,-0.345541325f,0.939459224f,-0.342660717f,0.940506071f,-0.339776884f,0.941544065f,-0.336889853f,0.942573198f,-0.333999651f,
0.943593458f,-0.331106306f,0.944604837f,-0.328209844f,0.945607325f,-0.325310292f,0.946600913f,-0.322407679f,0.947585591f,-0.319502031f,0.94856135f,-0.316593376f,
0.949528181f,-0.31368174f,0.950486074f,-0.310767153f,0.951435021f,-0.30784964f,0.952375013f,-0.30492923f,0.95330604f,-0.302005949f,0.954228095f,-0.299079826f,
0.955141168f,-0.296150888f,0.956045251f,-0.293219163f,0.956940336f,-0.290284677f,0.957826413f,-0.28734746f,0.958703475f,-0.284407537f,0.959571513f,-0.281464938f,
0.960430519f,-0.278519689f,0.961280486f,-0.275571819f,0.962121404f,-0.272621355f,0.962953267f,-0.269668326f,0.963776066f,-0.266712757f,0.964589793f,-0.263754679f,
0.965394442f,-0.260794118f,0.966190003f,-0.257831102f,0.966976471f,-0.25486566f,0.967753837f,-0.251897818f,0.968522094f,-0.248927606f,0.969281235f,-0.24595505f,
0.970031253f,-0.24298018f,0.970772141f,-0.240003022f,0.971503891f,-0.237023606f,0.972226497f,-0.234041959f,0.972939952f,-0.231058108f,0.97364425f,-0.228072083f,
0.974339383f,-0.225083911f,0.975025345f,-0.222093621f,0.97570213f,-0.21910124f,0.976369731f,-0.216106797f,0.977028143f,-0.21311032f,0.977677358f,-0.210111837f,
0.978317371f,-0.207111376f,0.978948175f,-0.204108966f,0.979569766f,-0.201104635f,0.980182136f,-0.198098411f,0.98078528f,-0.195090322f,0.981379193f,-0.192080397f,
0.981963869f,-0.189068664f,0.982539302f,-0.186055152f,0.983105487f,-0.183039888f,0.983662419f,-0.180022901f,0.984210092f,-0.17700422f,0.984748502f,-0.173983873f,
0.985277642f,-0.170961889f,0.985797509f,-0.167938295f,0.986308097f,-0.16491312f,0.986809402f,-0.161886394f,0.987301418f,-0.158858143f,0.987784142f,-0.155828398f,
0.988257568f,-0.152797185f,0.988721692f,-0.149764535f,0.98917651f,-0.146730474f,0.989622017f,-0.143695033f,0.99005821f,-0.140658239f,0.990485084f,-0.137620122f,
0.990902635f,-0.134580709f,0.99131086f,-0.131540029f,0.991709754f,-0.128498111f,0.992099313f,-0.125454983f,0.992479535f,-0.122410675f,0.992850414f,-0.119365215f,
0.993211949f,-0.116318631f,0.993564136f,-0.113270952f,0.99390697f,-0.110222207f,0.994240449f,-0.107172425f,0.994564571f,-0.104121634f,0.994879331f,-0.101069863f,
0.995184727f,-0.0980171403f,0.995480755f,-0.0949634953f,0.995767414f,-0.0919089565f,0.996044701f,-0.0888535526f,0.996312612f,-0.0857973123f,0.996571146f,-0.0827402645f,
0.996820299f,-0.079682438f,0.99706007f,-0.0766238614f,0.997290457f,-0.0735645636f,0.997511456f,-0.0705045734f,0.997723067f,-0.0674439196f,0.997925286f,-0.0643826309f,
0.998118113f,-0.0613207363f,0.998301545f,-0.0582582645f,0.998475581f,-0.0551952443f,0.998640218f,-0.0521317047f,0.998795456f,-0.0490676743f,0.998941293f,-0.0460031821f,
0.999077728f,-0.0429382569f,0.999204759f,-0.0398729276f,0.999322385f,-0.0368072229f,0.999430605f,-0.0337411719f,0.999529418f,-0.0306748032f,0.999618822f,-0.0276081458f,
0.999698819f,-0.0245412285f,0.999769405f,-0.0214740803f,0.999830582f,-0.0184067299f,0.999882347f,-0.0153392063f,0.999924702f,-0.0122715383f,0.999957645f,-0.00920375478f,
0.999981175f,-0.00613588465f,0.999995294f,-0.00306795676f,
};

DEV float bf_lo(uint32_t v) { return __uint_as_float(v << 16); }
DEV float bf_hi(uint32_t v) { return __uint_as_float(v & 0xffff0000u); }
DEV float bf16r(float f) { __bf16 b = (__bf16)f; return (float)b; }
DEV uint32_t pack_bf16(float lo, float hi) {
  uint32_t r;
  asm("v_cvt_pk_bf16_f32 %0, %1, %2" : "=v"(r) : "v"(lo), "v"(hi));
  return r;
}
DEV f2 unpack2(uint32_t p) { return f2{bf_lo(p), bf_hi(p)}; }
DEV f2 round2(f2 a) { return unpack2(pack_bf16(a.x, a.y)); }

DEV f2 add_mi(f2 a, f2 d) {
  f2 r; asm("v_pk_add_f32 %0, %1, %2 op_sel:[0,1] op_sel_hi:[1,0] neg_hi:[0,1]" : "=v"(r) : "v"(a), "v"(d)); return r;
}
DEV f2 add_pi(f2 a, f2 d) {
  f2 r; asm("v_pk_add_f32 %0, %1, %2 op_sel:[0,1] op_sel_hi:[1,0] neg_lo:[0,1]" : "=v"(r) : "v"(a), "v"(d)); return r;
}
template <int DIR> DEV f2 add_di(f2 a, f2 d) { return DIR < 0 ? add_mi(a, d) : add_pi(a, d); }
DEV f2 fma_mi(f2 d, f2 k, f2 c) {
  f2 r; asm("v_pk_fma_f32 %0, %1, %2, %3 op_sel:[1,0,0] op_sel_hi:[0,0,1] neg_hi:[1,0,0]" : "=v"(r) : "v"(d), "v"(k), "v"(c)); return r;
}
DEV f2 fma_pi(f2 d, f2 k, f2 c) {
  f2 r; asm("v_pk_fma_f32 %0, %1, %2, %3 op_sel:[1,0,0] op_sel_hi:[0,0,1] neg_lo:[1,0,0]" : "=v"(r) : "v"(d), "v"(k), "v"(c)); return r;
}
DEV f2 fma_pi_hi(f2 d, f2 k, f2 c) {
  f2 r; asm("v_pk_fma_f32 %0, %1, %2, %3 op_sel:[1,1,0] op_sel_hi:[0,1,1] neg_lo:[1,0,0]" : "=v"(r) : "v"(d), "v"(k), "v"(c)); return r;
}
DEV f2 fma_lo(f2 d, f2 k, f2 c) {
  f2 r; asm("v_pk_fma_f32 %0, %1, %2, %3 op_sel:[0,0,0] op_sel_hi:[1,0,1]" : "=v"(r) : "v"(d), "v"(k), "v"(c)); return r;
}
DEV f2 cmul(f2 a, f2 w) {
  f2 t, r;
  asm("v_pk_mul_f32 %0, %1, %2 op_sel:[0,0] op_sel_hi:[0,1]" : "=v"(t) : "v"(a), "v"(w));
  asm("v_pk_fma_f32 %0, %1, %2, %3 op_sel:[1,1,0] op_sel_hi:[1,0,1] neg_lo:[0,1,0]" : "=v"(r) : "v"(a), "v"(w), "v"(t));
  return r;
}
DEV f2 cmulc(f2 a, f2 w) {
  f2 t, r;
  asm("v_pk_mul_f32 %0, %1, %2 op_sel:[0,0] op_sel_hi:[0,1] neg_hi:[0,1]" : "=v"(t) : "v"(a), "v"(w));
  asm("v_pk_fma_f32 %0, %1, %2, %3 op_sel:[1,1,0] op_sel_hi:[1,0,1]" : "=v"(r) : "v"(a), "v"(w), "v"(t));
  return r;
}
DEV f2 cmul_s(f2 a, f2 w) {
  f2 t, r;
  asm("v_pk_mul_f32 %0, %1, %2 op_sel:[0,0] op_sel_hi:[0,1]" : "=v"(t) : "v"(a), "s"(w));
  asm("v_pk_fma_f32 %0, %1, %2, %3 op_sel:[1,1,0] op_sel_hi:[1,0,1] neg_lo:[0,1,0]" : "=v"(r) : "v"(a), "s"(w), "v"(t));
  return r;
}
DEV f2 cmulc_s(f2 a, f2 w) {
  f2 t, r;
  asm("v_pk_mul_f32 %0, %1, %2 op_sel:[0,0] op_sel_hi:[0,1] neg_hi:[0,1]" : "=v"(t) : "v"(a), "s"(w));
  asm("v_pk_fma_f32 %0, %1, %2, %3 op_sel:[1,1,0] op_sel_hi:[1,0,1]" : "=v"(r) : "v"(a), "s"(w), "v"(t));
  return r;
}
#define MUL7(NEG) asm("v_pk_mul_f32 %0, %7, %14 op_sel:[0,0] op_sel_hi:[0,1]" NEG "\n\tv_pk_mul_f32 %1, %8, %15 op_sel:[0,0] op_sel_hi:[0,1]" NEG \
  "\n\tv_pk_mul_f32 %2, %9, %16 op_sel:[0,0] op_sel_hi:[0,1]" NEG "\n\tv_pk_mul_f32 %3, %10, %17 op_sel:[0,0] op_sel_hi:[0,1]" NEG \
  "\n\tv_pk_mul_f32 %4, %11, %18 op_sel:[0,0] op_sel_hi:[0,1]" NEG "\n\tv_pk_mul_f32 %5, %12, %19 op_sel:[0,0] op_sel_hi:[0,1]" NEG \
  "\n\tv_pk_mul_f32 %6, %13, %20 op_sel:[0,0] op_sel_hi:[0,1]" NEG \
  : "=&v"(t[0]), "=&v"(t[1]), "=&v"(t[2]), "=&v"(t[3]), "=&v"(t[4]), "=&v"(t[5]), "=&v"(t[6]) \
  : "v"(a[1]), "v"(a[2]), "v"(a[3]), "v"(a[4]), "v"(a[5]), "v"(a[6]), "v"(a[7]), WC(w[0]), WC(w[1]), WC(w[2]), WC(w[3]), WC(w[4]), WC(w[5]), WC(w[6]))
#define FMA7(NEG) asm("v_pk_fma_f32 %0, %7, %14, %0 op_sel:[1,1,0] op_sel_hi:[1,0,1]" NEG "\n\tv_pk_fma_f32 %1, %8, %15, %1 op_sel:[1,1,0] op_sel_hi:[1,0,1]" NEG \
  "\n\tv_pk_fma_f32 %2, %9, %16, %2 op_sel:[1,1,0] op_sel_hi:[1,0,1]" NEG "\n\tv_pk_fma_f32 %3, %10, %17, %3 op_sel:[1,1,0] op_sel_hi:[1,0,1]" NEG \
  "\n\tv_pk_fma_f32 %4, %11, %18, %4 op_sel:[1,1,0] op_sel_hi:[1,0,1]" NEG "\n\tv_pk_fma_f32 %5, %12, %19, %5 op_sel:[1,1,0] op_sel_hi:[1,0,1]" NEG \
  "\n\tv_pk_fma_f32 %6, %13, %20, %6 op_sel:[1,1,0] op_sel_hi:[1,0,1]" NEG \
  : "+v"(t[0]), "+v"(t[1]), "+v"(t[2]), "+v"(t[3]), "+v"(t[4]), "+v"(t[5]), "+v"(t[6]) \
  : "v"(a[1]), "v"(a[2]), "v"(a[3]), "v"(a[4]), "v"(a[5]), "v"(a[6]), "v"(a[7]), WC(w[0]), WC(w[1]), WC(w[2]), WC(w[3]), WC(w[4]), WC(w[5]), WC(w[6]))
#define WC(x) "v"(x)
template <int DIR> DEV void ctw7(f2* a, const f2* w) {
  f2 t[7];
  if (DIR < 0) { MUL7(" neg_hi:[0,1]"); FMA7(""); } else { MUL7(""); FMA7(" neg_lo:[0,1,0]"); }
#pragma unroll
  for (int i = 0; i < 7; ++i) a[i + 1] = t[i];
}
#undef WC
#define WC(x) "s"(x)
template <int DIR> DEV void ctw7_s(f2* a, const f2* w) {
  f2 t[7];
  if (DIR < 0) { MUL7(" neg_hi:[0,1]"); FMA7(""); } else { MUL7(""); FMA7(" neg_lo:[0,1,0]"); }
#pragma unroll
  for (int i = 0; i < 7; ++i) a[i + 1] = t[i];
}
#undef WC
template <int OFF> DEV f2 ld8(uint32_t voff, const void* sbase) {
  f2 r; asm volatile("global_load_dwordx2 %0, %1, %2 offset:%3" : "=v"(r) : "v"(voff), "s"(sbase), "n"(OFF) : "memory"); return r;
}
template <int OFF> DEV uint32_t ld4(uint32_t voff, const void* sbase) {
  uint32_t r; asm volatile("global_load_dword %0, %1, %2 offset:%3" : "=v"(r) : "v"(voff), "s"(sbase), "n"(OFF) : "memory"); return r;
}
template <int OFF> DEV u32x4 ld16(uint32_t voff, const void* sbase) {
  u32x4 r; asm volatile("global_load_dwordx4 %0, %1, %2 offset:%3" : "=v"(r) : "v"(voff), "s"(sbase), "n"(OFF) : "memory"); return r;
}
DEV f2 sld8(const void* sbase, uint32_t soff_) {
  const uint32_t soff = __builtin_amdgcn_readfirstlane(soff_);
  f2 r; asm volatile("s_load_dwordx2 %0, %1, %2" : "=s"(r) : "s"(sbase), "s"(soff) : "memory"); return r;
}
DEV void spin8(f2* w) {
  asm volatile("s_waitcnt lgkmcnt(0)" : "+s"(w[0]), "+s"(w[1]), "+s"(w[2]), "+s"(w[3]), "+s"(w[4]), "+s"(w[5]), "+s"(w[6]), "+s"(w[7]) :: "memory");
}
DEV void pow7(f2 w, f2* o) {
  o[0] = w; o[1] = cmul(w, w); o[2] = cmul(o[1], w); o[3] = cmul(o[1], o[1]);
  o[4] = cmul(o[3], w); o[5] = cmul(o[2], o[2]); o[6] = cmul(o[3], o[2]);
}
template <int POL> DEV void st16p(void* p, u32x4 v) {
  if constexpr (POL == 0) *(__attribute__((address_space(1))) u32x4*)(uintptr_t)p = v;
  else asm volatile("global_store_dwordx4 %0, %1, off sc1\n\ts_nop 1" ::"v"(p), "v"(v) : "memory");
}
#define VMWAIT(N) asm volatile("s_waitcnt vmcnt(" #N ")" ::: "memory")
template <class T> DEV void pin(T& x) { asm volatile("" : "+v"(x)); }
template <int DIR> DEV f2 ctw(f2 a, f2 w) { return DIR < 0 ? cmulc(a, w) : cmul(a, w); }
template <int DIR> DEV f2 ctw_s(f2 a, f2 w) { return DIR < 0 ? cmulc_s(a, w) : cmul_s(a, w); }

template <int DIR> DEV void fft4(f2& a0, f2& a1, f2& a2, f2& a3) {
  f2 b0 = a0 + a2, b1 = a0 - a2, b2 = a1 + a3, d = a1 - a3;
  a0 = b0 + b2; a2 = b0 - b2; a1 = add_di<DIR>(b1, d); a3 = add_di<-DIR>(b1, d);
}
template <int DIR> DEV void fft8(f2* a) {
  const f2 R = f2{0.70710678118654752f, 0.70710678118654752f};
  f2 t0 = a[0] + a[4], t4 = a[0] - a[4];
  f2 t1 = a[1] + a[5], t5 = a[1] - a[5];
  f2 t2 = a[2] + a[6], t6 = a[2] - a[6];
  f2 t3 = a[3] + a[7], t7 = a[3] - a[7];
  f2 b0 = t0 + t2, b1 = t0 - t2, b2 = t1 + t3, d = t1 - t3;
  a[0] = b0 + b2; a[4] = b0 - b2; a[2] = add_di<DIR>(b1, d); a[6] = add_di<-DIR>(b1, d);
  f2 c0 = add_di<DIR>(t4, t6), c1 = add_di<-DIR>(t4, t6);
  f2 u5 = add_di<DIR>(t5, t5), v7 = add_di<-DIR>(t7, t7);
  f2 p = u5 - v7, q = u5 + v7;
  a[1] = c0 + R * p; a[5] = c0 - R * p;
  if (DIR < 0) { a[3] = fma_mi(q, R, c1); a[7] = fma_pi(q, R, c1); }
  else         { a[3] = fma_pi(q, R, c1); a[7] = fma_mi(q, R, c1); }
}
template <int DIR> DEV void fft5(f2* a) {
  const float c1 = 0.30901699437494742f, c2 = -0.80901699437494742f;
  const float s1 = 0.95105651629515357f, s2 = 0.58778525229247313f;
  f2 t1 = a[1] + a[4], t2 = a[2] + a[3], t3 = a[1] - a[4], t4 = a[2] - a[3];
  f2 a1 = a[0] + c1 * t1 + c2 * t2, a2 = a[0] + c2 * t1 + c1 * t2;
  f2 e1 = s1 * t3 + s2 * t4, e2 = s2 * t3 - s1 * t4;
  a[0] = a[0] + t1 + t2;
  a[1] = add_di<DIR>(a1, e1); a[4] = add_di<-DIR>(a1, e1);
  a[2] = add_di<DIR>(a2, e2); a[3] = add_di<-DIR>(a2, e2);
}

template <int CTRL> DEV float dpp_f(float v) {
  return __int_as_float(__builtin_amdgcn_update_dpp(0, __float_as_int(v), CTRL, 0xf, 0xf, true));
}
DEV float wave_sum(float v) {
  v += dpp_f<0xB1>(v);
  v += dpp_f<0x4E>(v);
  v += dpp_f<0x141>(v);
  v += dpp_f<0x140>(v);
  int iv = __float_as_int(v);
  float a = __int_as_float(__builtin_amdgcn_readlane(iv, 0));
  float b = __int_as_float(__builtin_amdgcn_readlane(iv, 16));
  float c = __int_as_float(__builtin_amdgcn_readlane(iv, 32));
  float d = __int_as_float(__builtin_amdgcn_readlane(iv, 48));
  return (a + b) + (c + d);
}
#define LDR(p) (*(f2*)(p))
#define WSYNC() __builtin_amdgcn_wave_barrier()
#define SB() __builtin_amdgcn_sched_barrier(0)

DEV float div640(float s) {
  const float q = s * (1.0f / 640.0f);
  return __builtin_fmaf(__builtin_fmaf(-q, 640.0f, s), 1.0f / 640.0f, q);
}
DEV void layernorm5(const f2* v, uint32_t* o) {
  f2 s2 = (v[0] + v[1]) + (v[2] + v[3]) + v[4];
  const float mean = div640(wave_sum(s2.x + s2.y));
  f2 q2 = f2{0.f, 0.f};
#pragma unroll
  for (int i = 0; i < 5; ++i) { f2 d = v[i] - mean; q2 += d * d; }
  const float var = div640(wave_sum(q2.x + q2.y));
  const float mb = bf16r(mean);
  const float den = bf16r(__builtin_amdgcn_sqrtf(bf16r(bf16r(var) + 1.0013580322265625e-05f)));
  const float rd = __builtin_amdgcn_rcpf(den);
#pragma unroll
  for (int i = 0; i < 5; ++i) { f2 e = round2(v[i] - mb) * rd; o[i] = pack_bf16(e.x, e.y); }
}

constexpr int ROWB = 2880;

DEV f2 fixup(f2 v, f2 w256) { return cmulc_s(f2{v.x, -v.y}, w256); }

template <int POL> DEV void store64_coalesced(const u32x4* o, u32x4* dst, char* wbuf, int lane, int nq) {
#pragma unroll
  for (int ff = 0; ff < 4; ++ff) *(u32x4*)(wbuf + lane * 80 + ff * 16) = o[ff];
  const uint64_t da = (uint64_t)(uintptr_t)dst;
  WSYNC();
#pragma unroll
  for (int q = 0; q < 4; ++q) {
    const int sl = 16 * q + (lane >> 2);
    const u32x4 v = *(const u32x4*)(wbuf + sl * 80 + (lane & 3) * 16);
    const uint32_t lo = __builtin_amdgcn_ds_bpermute(sl * 4, (int)(uint32_t)da);
    const uint32_t hi = __builtin_amdgcn_ds_bpermute(sl * 4, (int)(uint32_t)(da >> 32));
    if (q < nq) st16p<POL>((char*)(((uint64_t)hi << 32) | lo) + (lane & 3) * 16, v);
  }
  WSYNC();
}
DEV void retile_pack(const u32x2* v, u32x4* o) {
#pragma unroll
  for (int ff = 0; ff < 4; ++ff)
#pragma unroll
    for (int e = 0; e < 4; ++e) {
      uint32_t lo = v[2 * e][ff >> 1], hi = v[2 * e + 1][ff >> 1];
      o[ff][e] = (ff & 1) ? ((lo >> 16) | (hi & 0xffff0000u)) : ((lo & 0xffffu) | (hi << 16));
    }
}
DEV void retile_weights(int gid, const uint16_t* __restrict__ w1, const uint16_t* __restrict__ w2,
                        u32x4* __restrict__ W1f, u32x4* __restrict__ W2f, char* wbuf, int lane) {
  u32x2 v[8], y[8];
  const int fq = gid % 384, kh = gid / 384;
  const int dq = gid % 160, kg = gid / 160;
#pragma unroll
  for (int e = 0; e < 8; ++e) v[e] = *(const u32x2*)(w1 + (8 * kh + e) * 1536 + 4 * fq);
#pragma unroll
  for (int e = 0; e < 8; ++e) {
    int f = 32 * (kg >> 2) + (e < 4 ? 4 * (kg & 3) + e : 16 + 4 * (kg & 3) + e - 4);
    y[e] = *(const u32x2*)(w2 + f * 640 + 4 * dq);
  }
  u32x4 o[4];
  {
    int f = 4 * fq, j = f / 96, fr = f - 96 * j;
    retile_pack(v, o);
    store64_coalesced<1>(o, W1f + ((j * 20 + (kh >> 2)) * 6 + (fr >> 4)) * 64 + (kh & 3) * 16 + (fr & 15), wbuf, lane, 4);
  }
  {
    int d = 4 * dq, jd = d / 80, dr = d - 80 * jd;
    retile_pack(y, o);
    store64_coalesced<1>(o, W2f + ((jd * 48 + (kg >> 2)) * 5 + (dr >> 4)) * 64 + (kg & 3) * 16 + (dr & 15), wbuf, lane, 4);
  }
}

__global__ __launch_bounds__(512) void k1_kernel(const uint32_t* __restrict__ x, const uint16_t* __restrict__ wr,
                                                 const uint16_t* __restrict__ wi, f2* __restrict__ T,
                                                 u32x4* __restrict__ Wt, u32x4* __restrict__ Wx) {
  __shared__ __attribute__((aligned(16))) char smem[8 * ROWB + 3 * 5120];
  char* wtbuf = smem + 8 * ROWB;
  const int tid = threadIdx.x, wave = tid >> 6, lane = tid & 63;
  const int s0 = blockIdx.x;
  const int s = s0 + 256 * wave;
  const int k1 = lane >> 3, lo = lane & 7;
  uint32_t raw[5];
  {
    const uint32_t vo = (s * 320 + lane) * 4;
    raw[0] = ld4<0>(vo, x); raw[1] = ld4<256>(vo, x); raw[2] = ld4<512>(vo, x); raw[3] = ld4<768>(vo, x); raw[4] = ld4<1024>(vo, x);
  }
  u32x4 tl = ld16<0>(lane * 16, g_twf);
  f2 twd[8];
#pragma unroll
  for (int i = 0; i < 8; ++i) twd[i] = sld8(g_twf, (TW2K + s0 * (i + 1)) * 8);
  f2 twa[4], twb[7];
  char* rb = smem + wave * ROWB;
  u32x4 fw[4];
  const int ft = tid - 320, fi = ft & 7, fg = ft >> 3, frr = blockIdx.x & 7;
  const int fup = 8 * (blockIdx.x >> 3) + fi, fu = 8 * fup + frr, fmu = (2048 - fu) & 2047;
  const int wave_u = __builtin_amdgcn_readfirstlane(wave);
  uint32_t cpf = 0;
  {
    f2 z[5];
    if (wave_u >= 5) {
      const bool reg = ft < 160;
      const int g2 = reg ? fg : 0;
      const uint16_t* xa = (fg & 1) ? wi : wr;
      const uint16_t* p0 = reg ? wr + fu * 321 + 8 * g2 : xa + fmu * 321;
      const uint16_t* p1 = reg ? wi + fu * 321 + 8 * g2 : xa + fu * 321 + 313;
      const uint16_t* p2 = reg ? wr + fmu * 321 + 313 - 8 * g2 : xa + fu * 321 + 160;
      const uint16_t* p3 = reg ? wi + fmu * 321 + 313 - 8 * g2 : xa + fmu * 321 + 160;
      fw[0] = *(const u32x4_a2*)p0; fw[1] = *(const u32x4_a2*)p1; fw[2] = *(const u32x4_a2*)p2; fw[3] = *(const u32x4_a2*)p3;
      const int bi = blockIdx.x >> 3;
      if (bi < 4) {
        uint64_t pc2, pc3;
        asm volatile("s_getpc_b64 s[40:41]\n\ts_add_u32 s40, s40, " K2SYM "@rel32@lo+4\n\ts_addc_u32 s41, s41, " K2SYM "@rel32@hi+12\n\ts_mov_b64 %0, s[40:41]" : "=s"(pc2) :: "s40", "s41", "scc");
        asm volatile("s_getpc_b64 s[40:41]\n\ts_add_u32 s40, s40, " K3SYM "@rel32@lo+4\n\ts_addc_u32 s41, s41, " K3SYM "@rel32@hi+12\n\ts_mov_b64 %0, s[40:41]" : "=s"(pc3) :: "s40", "s41", "scc");
        const int li = bi * 64 + lane, l3 = li - 64;
        const char* pa = wave_u == 5 ? (bi == 0 ? (const char*)pc2 + (lane < K2_LINES ? lane : K2_LINES - 1) * 128
                                                : (const char*)pc3 + (l3 < K3_LINES ? l3 : K3_LINES - 1) * 128)
                       : wave_u == 6 ? (const char*)g_twf + (li < 210 ? li : 209) * 128
                                     : (const char*)T + (size_t)(li < 240 ? li : 239) * 131072;
        cpf = *(const __attribute__((address_space(1))) uint32_t*)(uintptr_t)pa;
      }
    }
    if (wave_u >= 5) VMWAIT(4); else VMWAIT(0);
#pragma unroll
    for (int q = 0; q < 5; ++q) { pin(raw[q]); z[q] = unpack2(raw[q]); }
    uint32_t ln[5];
    layernorm5(z, ln);
#pragma unroll
    for (int q = 0; q < 5; ++q) z[q] = unpack2(ln[q]);
    fft5<-1>(z);
    pin(tl);
    spin8(twd);
    {
      f32x4 t4 = __builtin_bit_cast(f32x4, tl);
      twa[0] = f2{t4.x, t4.y}; twa[1] = cmul(twa[0], twa[0]); twa[2] = cmul(twa[1], twa[0]); twa[3] = cmul(twa[1], twa[1]);
      pow7(f2{t4.z, t4.w}, twb);
    }
    asm volatile("" ::: "memory");
#pragma unroll
    for (int i = 1; i < 5; ++i) z[i] = cmulc(z[i], twa[i - 1]);
#pragma unroll
    for (int i = 0; i < 5; ++i) *(f2*)(rb + i * 576 + lane * 8) = z[i];
  }
  WSYNC();
  if (lane < 40) {
    f2 a[8];
#pragma unroll
    for (int l1 = 0; l1 < 8; ++l1) a[l1] = LDR(rb + k1 * 576 + (8 * l1 + lo) * 8);
    fft8<-1>(a);
    ctw7<-1>(a, twb);
    WSYNC();
#pragma unroll
    for (int m1 = 0; m1 < 8; ++m1) *(f2*)(rb + k1 * 576 + m1 * 72 + lo * 8) = a[m1];
    WSYNC();
#pragma unroll
    for (int l2 = 0; l2 < 8; ++l2) a[l2] = LDR(rb + k1 * 576 + lo * 72 + l2 * 8);
    fft8<-1>(a);
    WSYNC();
#pragma unroll
    for (int m2 = 0; m2 < 8; ++m2) *(f2*)(rb + (k1 + 5 * lo + 40 * m2) * 8) = a[m2];
  }
  __syncthreads();
  if (tid < 320) {
    const int pos = tid & 15, side = pos & 1, m = (tid >> 4) * 8 + (pos >> 1);
    const int k = (m == 0) ? (side ? 160 : 0) : (side ? 320 - m : m);
    const uint32_t sgn = side ? 0x80000000u : 0u;
    f2 a[8];
#pragma unroll
    for (int j = 0; j < 8; ++j) {
      a[j] = LDR(smem + j * ROWB + k * 8);
      a[j].y = __uint_as_float(__float_as_uint(a[j].y) ^ sgn);
    }
    fft8<-1>(a);
    ctw7_s<-1>(a, twd);
    if (wave_u == 0) {
      f2 gq[8];
#pragma unroll
      for (int rr = 1; rr < 8; ++rr) {
        if (rr == 4) continue;
        f2 fx = fixup(a[8 - rr], twd[7]);
        gq[rr] = f2{dpp_f<0xB1>(fx.x), dpp_f<0xB1>(fx.y)};
      }
#pragma unroll
      for (int rr = 1; rr < 8; ++rr) {
        if (rr == 4) continue;
        if ((tid == 0 && rr >= 5) || (tid == 1 && rr <= 3)) a[rr] = gq[rr];
      }
    }
    f2* dst = T + (((tid >> 4) * 8 + 4 * side) * 256 + s0) * 16 + (pos & ~1);
#pragma unroll
    for (int q = 0; q < 4; ++q) {
      const f2 snd = side ? a[q] : a[q + 4];
      const f2 rcv = f2{dpp_f<0xB1>(snd.x), dpp_f<0xB1>(snd.y)};
      const f2 lo = side ? rcv : a[q], hi = side ? a[q + 4] : rcv;
      st16p<0>(dst + q * 4096, __builtin_bit_cast(u32x4, f32x4{lo.x, lo.y, hi.x, hi.y}));
    }
  }
  else {
    u32x4 o[4];
#pragma unroll
    for (int jj = 0; jj < 4; ++jj) {
      uint32_t r = fw[0][jj], im = fw[1][jj], qr = fw[2][3 - jj], qi = fw[3][3 - jj];
      o[jj][0] = (r & 0xffffu) | (im << 16);
      o[jj][1] = (qr >> 16) | (qi & 0xffff0000u);
      o[jj][2] = (r >> 16) | (im & 0xffff0000u);
      o[jj][3] = (qr & 0xffffu) | (qi << 16);
    }
    u32x4* dst = Wt + (((ft < 160 ? fg : 0) * 8 + frr) * 256 + fup) * 4;
    store64_coalesced<0>(o, dst, wtbuf + (wave_u - 5) * 5120, lane, wave_u == 7 ? 2 : 4);
  }
  if (ft >= 160 && ft < 176) {
    u32x2 o = u32x2{(fw[0][0] & 0xffffu) | (fw[1][3] & 0xffff0000u), (fw[2][0] & 0xffffu) | (fw[3][0] << 16)};
    ((u32x2*)Wx)[(frr * 256 + fup) * 2 + (fg & 1)] = o;
  }
  asm volatile("" ::"v"(cpf));
}

constexpr int K2_AS = 1152, K2_PS = 4624, K2_COEF = 8 * K2_PS;
__global__ __launch_bounds__(512) void k2_kernel(const f2* __restrict__ T, f2* __restrict__ T2,
                                                 const u32x4* __restrict__ Wt, const u32x4* __restrict__ Wx,
                                                 const uint16_t* __restrict__ w1, const uint16_t* __restrict__ w2,
                                                 u32x4* __restrict__ W1f, u32x4* __restrict__ W2f,
                                                 unsigned* __restrict__ flags) {
  __shared__ __attribute__((aligned(16))) char smem[K2_COEF + 9 * 4096];
  const int tid = threadIdx.x, lane = tid & 63;
  if (tid >= 510) flags[((tid - 510) * 256 + blockIdx.x) * 64] = 0u;
  if (blockIdx.x >= 160) {
    __builtin_amdgcn_s_sleep(32);
    if (tid < 320) retile_weights((blockIdx.x - 160) * 320 + tid, w1, w2, W1f, W2f, smem + (tid >> 6) * 5120, lane);
    return;
  }
  const int wave = __builtin_amdgcn_readfirstlane(tid >> 6);
  const int chunk = blockIdx.x, g = chunk >> 3, rr = chunk & 7;
  const int pp = tid & 7, c = tid >> 3;
  const f2* Tin = T + (size_t)chunk * 4096;
  u32x4 ld[4];
  ld[0] = ld16<0>(tid * 16, Tin); ld[1] = ld16<0>(tid * 16 + 8192, Tin);
  ld[2] = ld16<0>(tid * 16 + 16384, Tin); ld[3] = ld16<0>(tid * 16 + 24576, Tin);
  f2 tw1[3], tw2[7];
  const int a1 = lane >> 4, xx = (lane >> 1) & 7, side = lane & 1;
  u32x4 tl = ld16<TL2 * 8>(tid * 16, g_twf);
  const int m = 8 * g + wave;
  const bool special = (m == 0) && (rr == 0 || rr == 4);
  const int k0 = m != 0 ? m : (special ? (side ? 160 : 0) : (rr < 4 ? 0 : 160));
  f2 th = ld8<0>((TW640 + k0) * 8, g_twf);
  const int up = tid >> 1, hp = tid & 1;
  u32x4 wq0 = ld16<0>(tid * 32, Wt + chunk * 1024), wq1 = ld16<16>(tid * 32, Wt + chunk * 1024);
  u32x4 ex = ld16<0>(up * 16, Wx + rr * 256);

  {
    f2 p[4], q[4];
    VMWAIT(4);
    pin(tl);
    {
      f32x4 t4 = __builtin_bit_cast(f32x4, tl);
      tw1[0] = f2{t4.x, t4.y}; tw1[1] = cmul(tw1[0], tw1[0]); tw1[2] = cmul(tw1[1], tw1[0]);
      pow7(f2{t4.z, t4.w}, tw2);
    }
#pragma unroll
    for (int i = 0; i < 4; ++i) {
      pin(ld[i]);
      f32x4 t = __builtin_bit_cast(f32x4, ld[i]);
      p[i] = f2{t.x, t.y}; q[i] = f2{t.z, t.w};
    }
    fft4<-1>(p[0], p[1], p[2], p[3]);
    fft4<-1>(q[0], q[1], q[2], q[3]);
#pragma unroll
    for (int i = 1; i < 4; ++i) { p[i] = cmulc(p[i], tw1[i - 1]); q[i] = cmulc(q[i], tw1[i - 1]); }
#pragma unroll
    for (int i = 0; i < 4; ++i)
      *(f32x4*)(smem + pp * K2_PS + i * K2_AS + c * 16) = f32x4{p[i].x, p[i].y, q[i].x, q[i].y};
  }
  VMWAIT(0);
  pin(th); pin(wq0); pin(wq1); pin(ex);
  const f2 sc_ = side ? f2{th.y, -th.x} : f2{-th.y, th.x};
  {
    const float sc = 0.5f / (2048.0f * 320.0f);
    const f2 scp = f2{sc, sc}, scc = f2{sc, -sc};
    f32x4* coef = (f32x4*)(smem + K2_COEF) + (4 * hp) * 256 + up;
#pragma unroll
    for (int i = 0; i < 4; ++i) {
      const u32x4 w = (i < 2) ? wq0 : wq1;
      f2 G0 = unpack2(w[2 * (i & 1)]) * scp, G1 = unpack2(w[2 * (i & 1) + 1]) * scc;
      if (i == 0 && g == 0) {
        f2 H0 = f2{bf_lo(ex[1]), bf_lo(ex[3])} * scp, H1 = f2{bf_hi(ex[1]), bf_hi(ex[3])} * scc;
        if (hp == 0) {
          f2 S = H0 + H1, D = H0 - H1;
          coef[8 * 256] = f32x4{S.x, S.y, D.x, D.y};
          if (rr <= 4) { G0 = 0.5f * (G0 + f2{bf_lo(ex[0]), bf_lo(ex[2])} * scc); G1 = 0.5f * (G1 + f2{bf_hi(ex[0]), bf_hi(ex[2])} * scp); }
          else { G0 = H0; G1 = H1; }
        }
      }
      f2 S = G0 + G1, D = G0 - G1;
      coef[i * 256] = f32x4{S.x, S.y, D.x, D.y};
    }
  }
  __syncthreads();
  char* wb_ = smem + wave * K2_PS;
  char* e1 = wb_ + a1 * K2_AS + xx * 16 + side * 8;
  char* e2 = wb_ + a1 * K2_AS + xx * 144 + side * 8;
  f2 a[8];
#pragma unroll
  for (int c1 = 0; c1 < 8; ++c1) a[c1] = LDR(e1 + c1 * 128);
  fft8<-1>(a);
  ctw7<-1>(a, tw2);
  WSYNC();
#pragma unroll
  for (int b1 = 0; b1 < 8; ++b1) *(f2*)(e1 + b1 * 144) = a[b1];
  WSYNC();
#pragma unroll
  for (int c2 = 0; c2 < 8; ++c2) a[c2] = LDR(e2 + c2 * 16);
  fft8<-1>(a);
  const int e = a1 + 4 * xx;
  f2 pv[8];
#pragma unroll
  for (int b2 = 0; b2 < 8; ++b2) pv[b2] = f2{dpp_f<0xB1>(a[b2].x), dpp_f<0xB1>(a[b2].y)};
  if (special) {
    WSYNC();
    if (side == 0) {
#pragma unroll
      for (int b2 = 0; b2 < 8; ++b2) *(f2*)(wb_ + (e + 32 * b2) * 8) = a[b2];
    }
    WSYNC();
    if (side == 0) {
#pragma unroll
      for (int b2 = 0; b2 < 8; ++b2) {
        f2 t = LDR(wb_ + ((-(e + 32 * b2) - (rr ? 1 : 0)) & 255) * 8);
        pv[b2] = f2{t.x, -t.y};
      }
    }
  }
  {
    const f32x4* coef = (const f32x4*)(smem + K2_COEF) + ((special && side) ? 8 : wave) * 256 + e;
#pragma unroll
    for (int b2 = 0; b2 < 8; ++b2) {
      f32x4 cf = coef[b2 * 32];
      f2 S = f2{cf.x, cf.y}, D = f2{cf.z, cf.w};
      f2 al = fma_lo(D, sc_, S);
      f2 ee = cmul(D, pv[b2]);
      f2 t = cmul(al, a[b2]);
      a[b2] = fma_pi_hi(ee, sc_, t);
    }
  }
  fft8<1>(a);
#pragma unroll
  for (int c2 = 0; c2 < 8; ++c2) *(f2*)(e2 + c2 * 16) = a[c2];
  WSYNC();
#pragma unroll
  for (int b1 = 0; b1 < 8; ++b1) a[b1] = LDR(e1 + b1 * 144);
  ctw7<1>(a, tw2);
  fft8<1>(a);
  WSYNC();
#pragma unroll
  for (int c1 = 0; c1 < 8; ++c1) *(f2*)(e1 + c1 * 128) = a[c1];
  __syncthreads();
  {
    f2 p[4], q[4];
#pragma unroll
    for (int i = 0; i < 4; ++i) {
      f32x4 t = *(f32x4*)(smem + pp * K2_PS + i * K2_AS + c * 16);
      p[i] = f2{t.x, t.y}; q[i] = f2{t.z, t.w};
    }
#pragma unroll
    for (int i = 1; i < 4; ++i) { p[i] = cmul(p[i], tw1[i - 1]); q[i] = cmul(q[i], tw1[i - 1]); }
    fft4<1>(p[0], p[1], p[2], p[3]);
    fft4<1>(q[0], q[1], q[2], q[3]);
    f32x4* Tout = (f32x4*)(T2 + (size_t)chunk * 4096);
#pragma unroll
    for (int i = 0; i < 4; ++i) st16p<1>(Tout + i * 512 + tid, __builtin_bit_cast(u32x4, f32x4{p[i].x, p[i].y, q[i].x, q[i].y}));
  }
}

constexpr int G1_DH = 2;
constexpr int W2_AG = 44;
constexpr int G1_PRO = 5 * G1_DH;
struct GCtx {
  const u32x4* w1src; const u32x4* asrc; const u32x4* w2src; const u32x4* hsrc; f32x4* lds; int wave, lane; uint32_t vo; int rot1, rot2;
};
#define FRAG_LD(CON, dst, base, Q, MOD) asm volatile("global_load_dwordx4 %0, %1, %2 offset:%3" MOD : CON(dst) : "v"(c.vo), "s"((base) + ((Q) & ~3) * 64), "n"(((Q) & 3) * 1024))
constexpr int g1_issued(int h, int kk) {
  int n = G1_PRO;
  for (int q = 0; q < h * 5 + kk; ++q) n += ((q / 5) + G1_DH <= 7 ? 1 : 0);
  return n;
}
constexpr int g1_need(int h, int kk) {
  return h < G1_DH ? 5 * h + kk : g1_issued(h - G1_DH, kk);
}
template <int N> DEV void vwait1(u32x4& a) { asm volatile("s_waitcnt vmcnt(%1)" : "+v"(a) : "n"(N) : "memory"); }
template <int H, int KK> DEV void g1_aissue(const GCtx& c, u32x4 (&a)[8][5]) {
  const int mt = 2 * ((c.rot1 + (H >> 1)) & 3) + (H & 1);
  FRAG_LD("=v", a[H][KK], c.asrc + mt * 1280, KK, " sc1");
}
template <int Q> DEV void w2_issue(const GCtx& c, u32x4 (&w2)[60]) {
  static_assert(Q < 60, "w2");
  if constexpr (Q < W2_AG) FRAG_LD("=a", w2[Q], c.w2src, Q, "");
  else FRAG_LD("=v", w2[Q], c.w2src, Q, "");
}
template <int Q, int QE> DEV void w2_issue_range(const GCtx& c, u32x4 (&w2)[60]) {
  w2_issue<Q>(c, w2);
  if constexpr (Q + 1 < QE) w2_issue_range<Q + 1, QE>(c, w2);
}
template <int Q, int QE> DEV void wld_(const GCtx& c, u32x4 (&w1)[5][6], u32x4 (&w2)[60]) {
  if constexpr (Q < 30) FRAG_LD("=v", w1[Q / 6][Q % 6], c.w1src, Q, "");
  else FRAG_LD("=a", w2[Q - 30], c.w2src, Q - 30, "");
  if constexpr (Q + 1 < QE) wld_<Q + 1, QE>(c, w1, w2);
}
template <int Q, int QE> DEV void wld(const GCtx& c, u32x4 (&w1)[5][6], u32x4 (&w2)[60]) {
  wld_<Q, QE>(c, w1, w2);
}
template <int Q> DEV void w2_pin(u32x4 (&w2)[60]) {
  if constexpr (Q < W2_AG) asm volatile("" : "+a"(w2[Q])); else asm volatile("" : "+v"(w2[Q]));
  if constexpr (Q + 1 < 60) w2_pin<Q + 1>(w2);
}
template <int H> DEV void g1_prologue2(const GCtx& c, u32x4 (&a)[8][5]) {
  g1_aissue<H, 0>(c, a); g1_aissue<H, 1>(c, a); g1_aissue<H, 2>(c, a); g1_aissue<H, 3>(c, a); g1_aissue<H, 4>(c, a);
  if constexpr (H + 1 < G1_DH) g1_prologue2<H + 1>(c, a);
}
#define MFMA16(A, B, C) __builtin_amdgcn_mfma_f32_16x16x32_bf16(__builtin_bit_cast(bf16x8, A), __builtin_bit_cast(bf16x8, B), C, 0, 0, 0)
#define LBAR() do { asm volatile("s_waitcnt lgkmcnt(0)" ::: "memory"); __builtin_amdgcn_s_barrier(); asm volatile("" ::: "memory"); } while (0)
template <int H, int N> DEV void g1_side(const GCtx& c, f32x4 (&acc)[2][6]) {
  if constexpr (H >= 1 && H <= 6) {
    if constexpr (N < 6) c.lds[((((H - 1) & 1) * 4 + c.wave) * 6 + N) * 64 + c.lane] = acc[(H - 1) & 1][N];
    else acc[(H + 1) & 1][N - 6] = c.lds[((((H - 1) & 1) * 4 + ((c.wave + 1) & 3)) * 6 + N - 6) * 64 + c.lane];
  }
}
struct G1Epi { const u32x2* bv; u32x4* dst; u32x4 pk; };
template <int N> DEV void g1_epi_unit(const f32x4* acc, G1Epi& e) {
  constexpr int tp = N >> 2, q = N & 3;
  const u32x2 bq = e.bv[2 * tp + (q >> 1)];
  const f32x4 av = acc[2 * tp + (q >> 1)];
  f2 hh = round2(f2{av[2 * (q & 1)], av[2 * (q & 1) + 1]}) + unpack2((q & 1) ? bq.y : bq.x);
  uint32_t hp = pack_bf16(hh.x, hh.y), rp;
  asm("v_pk_max_i16 %0, %1, 0" : "=v"(rp) : "v"(hp));
  f2 r = unpack2(rp);
  r = r * r;
  e.pk[q] = pack_bf16(r.x, r.y);
  if constexpr (q == 3)
    asm volatile("global_store_dwordx4 %0, %1, off sc1\n\ts_nop 1" ::"v"(e.dst + tp * 64), "v"(e.pk) : "memory");
}
template <int N> DEV void g1_epi_from(const f32x4* acc, G1Epi& e) {
  g1_epi_unit<N>(acc, e);
  if constexpr (N < 11) g1_epi_from<N + 1>(acc, e);
}
template <int H, int KK, int S> DEV void g1_slot(const GCtx& c, f32x4 (&acc)[2][6], G1Epi& e) {
  if constexpr (H == 7 && KK < 4) g1_epi_unit<3 * KK + S>(acc[0], e);
  if constexpr (KK < 4) g1_side<H, 3 * KK + S>(c, acc);
}
template <int H, int KK> DEV void g1_kstep(const GCtx& c, u32x4 (&a)[8][5], u32x4 (&w1)[5][6], u32x4 (&w2)[60], G1Epi& e, f32x4 (&acc2)[2][6]) {
  f32x4 (&acc)[6] = acc2[H & 1];
  constexpr int allowed = g1_issued(H, KK) - g1_need(H, KK) - 1 + (H == 7 && KK >= 2 ? KK - 1 : 0);
  static_assert(allowed >= 0 && allowed < 64, "vmcnt");
  vwait1<allowed>(a[H][KK]);
  if constexpr (KK == 2 && H >= 1 && H <= 6) LBAR();
  if constexpr (H != 7) SB();
  acc[0] = MFMA16(w1[KK][0], a[H][KK], acc[0]);
  acc[1] = MFMA16(w1[KK][1], a[H][KK], acc[1]);
  if constexpr (H != 7) SB();
  g1_slot<H, KK, 0>(c, acc2, e);
  if constexpr (H + G1_DH <= 7) g1_aissue<H + G1_DH, KK>(c, a);
  if constexpr (H != 7) SB();
  acc[2] = MFMA16(w1[KK][2], a[H][KK], acc[2]);
  acc[3] = MFMA16(w1[KK][3], a[H][KK], acc[3]);
  if constexpr (H != 7) SB();
  g1_slot<H, KK, 1>(c, acc2, e);
  if constexpr (H != 7) SB();
  acc[4] = MFMA16(w1[KK][4], a[H][KK], acc[4]);
  acc[5] = MFMA16(w1[KK][5], a[H][KK], acc[5]);
  if constexpr (H != 7) SB();
  g1_slot<H, KK, 2>(c, acc2, e);
  if constexpr (H != 7) SB();
  if constexpr (KK < 4) g1_kstep<H, KK + 1>(c, a, w1, w2, e, acc2);
}
template <int H> DEV void g1_half(const GCtx& c, u32x4 (&a)[8][5], u32x4 (&w1)[5][6], u32x4 (&w2)[60], G1Epi& e, f32x4 (&acc)[2][6]) {
  g1_kstep<H, 0>(c, a, w1, w2, e, acc);
}
template <int TAU, int KK> DEV void h_issue(const GCtx& c, u32x4 (&hf)[3][12]) {
  const int mi = (c.rot2 + TAU) & 3;
  FRAG_LD("=v", hf[TAU % 3][KK], c.hsrc + mi * 3072, KK, " sc1");
}
template <int Q> DEV void h_prologue(const GCtx& c, u32x4 (&hf)[3][12]) {
  h_issue<Q / 12, Q % 12>(c, hf);
  if constexpr (Q + 1 < 24) h_prologue<Q + 1>(c, hf);
}
struct G2Epi { const u32x2* b2v; u32x2* hv; uint16_t* dst; const uint16_t* hsrc; };
template <int T> DEV void g2_epi_unit(const f32x4& sum, const G2Epi& e) {
  f2 o0 = round2(round2(f2{sum.x, sum.y}) + unpack2(e.b2v[T].x)) + unpack2(e.hv[T].x);
  f2 o1 = round2(round2(f2{sum.z, sum.w}) + unpack2(e.b2v[T].y)) + unpack2(e.hv[T].y);
  *(u32x2*)(e.dst + 16 * T) = u32x2{pack_bf16(o0.x, o0.y), pack_bf16(o1.x, o1.y)};
}
template <int HS, int KK> DEV void g2_kstep(const GCtx& c, u32x4 (&hf)[3][12], u32x4 (&w2)[60], f32x4 (&acc2)[2][3], const G2Epi& e) {
  constexpr int tau = HS >> 1, half = HS & 1;
  constexpr int no = half ? 3 : 2;
  f32x4 (&acc)[3] = acc2[HS & 1];
  if constexpr (KK == 5 && HS >= 1 && HS <= 6) LBAR();
  if constexpr (half == 0) {
    constexpr int issued = 24 + 12 * (tau < 2 ? tau : 2) + (tau < 2 ? KK : 0) + (tau == 2 ? (KK < 5 ? KK : 5) : 0) + (tau == 3 ? 5 : 0);
    vwait1<issued - (tau * 12 + KK) - 1>(hf[tau % 3][KK]);
    if constexpr (HS == 0 && KK == 0) w2_pin<0>(w2);
    SB();
    acc[0] = MFMA16(w2[KK * 5 + 0], hf[tau % 3][KK], acc[0]);
    acc[1] = MFMA16(w2[KK * 5 + 1], hf[tau % 3][KK], acc[1]);
    SB();
    if constexpr (tau < 2) h_issue<tau + 2, KK>(c, hf);
    if constexpr (tau == 2 && KK < 5)
      asm volatile("global_load_dwordx2 %0, %1, off sc1" : "=v"(e.hv[KK]) : "v"(e.hsrc + 16 * KK) : "memory");
    SB();
    acc[2] = MFMA16(w2[KK * 5 + 2], hf[tau % 3][KK], acc[2]);
  } else {
    SB();
    acc[0] = MFMA16(w2[KK * 5 + 3], hf[tau % 3][KK], acc[0]);
    acc[1] = MFMA16(w2[KK * 5 + 4], hf[tau % 3][KK], acc[1]);
  }
  SB();
  if constexpr (HS >= 1 && HS <= 6) {
    if constexpr (KK < no) c.lds[((((HS - 1) & 1) * 4 + c.wave) * 3 + KK) * 64 + c.lane] = acc2[(HS - 1) & 1][KK];
    if constexpr (KK >= 5 && KK < 5 + no)
      acc2[(HS + 1) & 1][KK - 5] = c.lds[((((HS - 1) & 1) * 4 + ((c.wave + 1) & 3)) * 3 + KK - 5) * 64 + c.lane];
  }
  if constexpr (HS == 7 && (KK & 3) == 1) g2_epi_unit<(KK >> 2)>(acc2[0][KK >> 2], e);
  SB();
  if constexpr (KK < 11) g2_kstep<HS, KK + 1>(c, hf, w2, acc2, e);
}
template <int HS> DEV void g2_half(const GCtx& c, u32x4 (&hf)[3][12], u32x4 (&w2)[60], f32x4 (&acc)[2][3], const G2Epi& e) {
  g2_kstep<HS, 0>(c, hf, w2, acc, e);
}
#define ST4_SC1(ptr, v) asm volatile("global_store_dword %0, %1, off sc1\n\ts_nop 1" ::"v"(ptr), "v"(v) : "memory")
#define ST16_SC1(ptr, v) asm volatile("global_store_dwordx4 %0, %1, off sc1\n\ts_nop 1" ::"v"(ptr), "v"(v) : "memory")
#define ASM_LOAD8(dst, ptr) asm volatile("global_load_dwordx2 %0, %1, off" : "=v"(dst) : "v"(ptr) : "memory")
DEV void group_publish(unsigned* gf, int j, int tid) {
  asm volatile("s_waitcnt vmcnt(0)" ::: "memory");
  __builtin_amdgcn_s_barrier();
  asm volatile("" ::: "memory");
  if (tid == 0) ST4_SC1(gf + j * 64, 1u);
}
DEV void group_wait(unsigned* gf, int wave, int lane) {
  if (wave == 0) {
    for (unsigned sp = 0; sp < 400000u; ++sp) {
      unsigned f;
      asm volatile("global_load_dword %0, %1, off sc1\n\ts_waitcnt vmcnt(0)" : "=&v"(f) : "v"(gf + (lane & 15) * 64) : "memory");
      if (__all(f == 1u)) break;
      __builtin_amdgcn_s_sleep(1);
    }
  }
  __builtin_amdgcn_s_barrier();
  asm volatile("" ::: "memory");
}

__global__ __launch_bounds__(256, 1) void k345_kernel(const uint32_t* __restrict__ x, const f2* __restrict__ T2,
                                                      uint32_t* hbuf, u32x4* L2f, const u32x4* __restrict__ W1f,
                                                      const uint16_t* __restrict__ b1, u32x4* Hf,
                                                      const u32x4* __restrict__ W2f, const uint16_t* __restrict__ b2,
                                                      uint16_t* __restrict__ out, unsigned* flags) {
  __shared__ f32x4 lds[2 * 4 * 6 * 64];
  char* smem = (char*)lds;
  const int tid = threadIdx.x, lane = tid & 63;
  const int wave = __builtin_amdgcn_readfirstlane(tid >> 6);
  const int b = blockIdx.x, xcd = b & 7, idx = b >> 3;
  const int i = (xcd >> 1) * 4 + (idx >> 3), j = (xcd & 1) * 8 + (idx & 7);
  const int i5 = 2 * i + (j & 1), jd = j >> 1;
  const int h = lane >> 4;
  const int rot1 = wave, rot2 = wave;
  const int wl1 = (rot1 + 3) & 3, wl = (rot2 + 3) & 3;
  u32x4 w2[60], w1[5][6];
  GCtx c{W1f + ((size_t)j * 20 + 5 * wave) * 6 * 64, L2f + ((size_t)(8 * i) * 20 + 5 * wave) * 64,
         W2f + ((size_t)jd * 48 + 12 * wave) * 5 * 64, Hf + ((size_t)(4 * i5) * 48 + 12 * wave) * 64,
         lds, wave, lane, (uint32_t)lane * 16u, rot1, rot2};
  u32x2 bv[6], b2v[5], hv[5];
  {
    const int s0 = 16 * i + j;
    const int k1 = lane >> 3, lo = lane & 7;
    f2 twd[8];
#pragma unroll
    for (int q = 0; q < 8; ++q) twd[q] = sld8(g_twf, (TW2K + s0 * (q + 1)) * 8);
    f2 a[2][8];
    const int tB = 256 + 16 * wave + (lane & 15);
    {
      const uint32_t vo = (((tid >> 4) * 8 * 256 + s0) * 16 + (tid & 15)) * 8;
#pragma unroll
      for (int rr = 0; rr < 8; ++rr) {
        bool sp = (tid == 0 && rr >= 5) || (tid == 1 && rr >= 1 && rr <= 3);
        uint32_t o = vo + rr * 32768;
        if (sp) o = (((8 - rr) * 256 + s0) * 16 + (tid ^ 1)) * 8;
        a[0][rr] = ld8<0>(o, T2);
      }
      const uint32_t vb = (((tB >> 4) * 8 * 256 + s0) * 16 + (tB & 15)) * 8;
#pragma unroll
      for (int rr = 0; rr < 8; ++rr) a[1][rr] = ld8<0>(vb + rr * 32768, T2);
    }
    uint32_t raw[2][5];
#pragma unroll
    for (int r = 0; r < 2; ++r) {
      const uint32_t vo = ((s0 + 256 * (2 * wave + r)) * 320 + lane) * 4;
      raw[r][0] = ld4<0>(vo, x); raw[r][1] = ld4<256>(vo, x); raw[r][2] = ld4<512>(vo, x); raw[r][3] = ld4<768>(vo, x); raw[r][4] = ld4<1024>(vo, x);
    }
    u32x4 tl = ld16<0>(lane * 16, g_twf);
#pragma unroll
    for (int t = 0; t < 6; ++t) ASM_LOAD8(bv[t], b1 + 96 * j + 16 * t + 4 * h);
#pragma unroll
    for (int t = 0; t < 5; ++t) ASM_LOAD8(b2v[t], b2 + 80 * jd + 16 * t + 4 * h);
    wld<0, 12>(c, w1, w2);
    VMWAIT(34);
    spin8(twd);
#pragma unroll
    for (int rr = 0; rr < 8; ++rr) { pin(a[0][rr]); pin(a[1][rr]); }
    if (wave == 0) {
#pragma unroll
      for (int rr = 1; rr < 8; ++rr) {
        if (rr == 4) continue;
        bool sp = (tid == 0 && rr >= 5) || (tid == 1 && rr >= 1 && rr <= 3);
        f2 fx = fixup(a[0][rr], twd[7]);
        if (sp) a[0][rr] = fx;
      }
    }
    ctw7_s<1>(a[0], twd);
    wld<12, 14>(c, w1, w2);
    ctw7_s<1>(a[1], twd);
    wld<14, 16>(c, w1, w2);
    fft8<1>(a[0]);
    wld<16, 18>(c, w1, w2);
    fft8<1>(a[1]);
    wld<18, 20>(c, w1, w2);
    char* scratch = smem + 8 * ROWB;
#pragma unroll
    for (int r = 0; r < 2; ++r) {
      const int t = r ? tB : tid;
      const int pos = t & 15, side = pos & 1, m = (t >> 4) * 8 + (pos >> 1);
      const int k = (m == 0) ? (side ? 160 : 0) : (side ? 320 - m : m);
      const uint32_t sgn = side ? 0x80000000u : 0u;
      char* d = (r == 0 || lane < 16) ? smem + k * 8 : scratch + lane * 8;
#pragma unroll
      for (int jj = 0; jj < 8; ++jj) {
        a[r][jj].y = __uint_as_float(__float_as_uint(a[r][jj].y) ^ sgn);
        *(f2*)(d + jj * ROWB) = a[r][jj];
      }
    }
    VMWAIT(31);
#pragma unroll
    for (int q = 0; q < 5; ++q) { pin(raw[0][q]); pin(raw[1][q]); }
    pin(tl);
    wld<20, 23>(c, w1, w2);
    LBAR();
    char* rb0 = smem + (2 * wave) * ROWB;
    char* rbx = lane < 40 ? rb0 : scratch - 5 * 576;
#pragma unroll
    for (int r = 0; r < 2; ++r)
#pragma unroll
      for (int m2 = 0; m2 < 8; ++m2) a[r][m2] = LDR(rb0 + r * ROWB + (k1 + 5 * lo + 40 * m2) * 8);
    f2 twa[4], twb[7];
    {
      f32x4 t4 = __builtin_bit_cast(f32x4, tl);
      twa[0] = f2{t4.x, t4.y}; twa[1] = cmul(twa[0], twa[0]); twa[2] = cmul(twa[1], twa[0]); twa[3] = cmul(twa[1], twa[1]);
      pow7(f2{t4.z, t4.w}, twb);
    }
    wld<23, 26>(c, w1, w2);
    fft8<1>(a[0]);
    wld<26, 29>(c, w1, w2);
    fft8<1>(a[1]);
    WSYNC();
#pragma unroll
    for (int r = 0; r < 2; ++r)
#pragma unroll
      for (int l2 = 0; l2 < 8; ++l2) *(f2*)(rbx + r * ROWB + k1 * 576 + lo * 72 + l2 * 8) = a[r][l2];
    WSYNC();
    wld<29, 32>(c, w1, w2);
#pragma unroll
    for (int r = 0; r < 2; ++r)
#pragma unroll
      for (int m1 = 0; m1 < 8; ++m1) a[r][m1] = LDR(rbx + r * ROWB + k1 * 576 + m1 * 72 + lo * 8);
    wld<32, 35>(c, w1, w2);
    ctw7<1>(a[0], twb);
    wld<35, 37>(c, w1, w2);
    ctw7<1>(a[1], twb);
    wld<37, 40>(c, w1, w2);
    fft8<1>(a[0]);
    wld<40, 42>(c, w1, w2);
    fft8<1>(a[1]);
    WSYNC();
#pragma unroll
    for (int r = 0; r < 2; ++r)
#pragma unroll
      for (int l1 = 0; l1 < 8; ++l1) *(f2*)(rbx + r * ROWB + k1 * 576 + (8 * l1 + lo) * 8) = a[r][l1];
    WSYNC();
    wld<42, 45>(c, w1, w2);
    uint32_t ln[2][5];
    f2 z[2][5];
#pragma unroll
    for (int r = 0; r < 2; ++r)
#pragma unroll
      for (int q = 0; q < 5; ++q) z[r][q] = LDR(rb0 + r * ROWB + q * 576 + lane * 8);
    wld<45, 48>(c, w1, w2);
#pragma unroll
    for (int r = 0; r < 2; ++r) {
#pragma unroll
      for (int q = 1; q < 5; ++q) z[r][q] = cmul(z[r][q], twa[q - 1]);
      fft5<1>(z[r]);
      if (r == 0) wld<48, 51>(c, w1, w2); else wld<51, 54>(c, w1, w2);
    }
    uint32_t hp[2][5];
#pragma unroll
    for (int r = 0; r < 2; ++r)
#pragma unroll
      for (int q = 0; q < 5; ++q) {
        f2 hh = unpack2(raw[r][q]) + round2(z[r][q]);
        hp[r][q] = pack_bf16(hh.x, hh.y);
        z[r][q] = unpack2(hp[r][q]);
      }
    wld<54, 57>(c, w1, w2);
    layernorm5(z[0], ln[0]);
    layernorm5(z[1], ln[1]);
    wld<57, 60>(c, w1, w2);
    WSYNC();
#pragma unroll
    for (int r = 0; r < 2; ++r)
#pragma unroll
      for (int q = 0; q < 5; ++q) *(uint32_t*)(rb0 + r * ROWB + (64 * q + lane) * 4) = ln[r][q];
#pragma unroll
    for (int r = 0; r < 2; ++r) {
      uint32_t* hrow = hbuf + (s0 + 256 * (2 * wave + r)) * 320 + lane;
#pragma unroll
      for (int q = 0; q < 5; ++q) ST4_SC1(hrow + 64 * q, hp[r][q]);
    }
    LBAR();
    {
      const int jj = tid & 7, kh = tid >> 3;
      u32x4* dst = L2f + (s0 >> 1) * 1280 + (kh >> 2) * 64 + (kh & 3) * 16 + 8 * (s0 & 1) + jj;
      const char* srcp = smem + jj * ROWB + kh * 16;
      u32x4 v0 = *(const u32x4*)srcp, v1 = *(const u32x4*)(srcp + 512);
      ST16_SC1(dst, v0);
      ST16_SC1(dst + 512, v1);
      if (tid < 128) {
        u32x4 v2 = *(const u32x4*)(srcp + 1024);
        ST16_SC1(dst + 1024, v2);
      }
    }
  }
  unsigned* gfa = flags + i * 16 * 64;
  unsigned* gfb = flags + (256 + i * 16) * 64;
  group_publish(gfa, j, tid);
#pragma unroll
  for (int q = 0; q < 30; ++q) pin(w1[q / 6][q % 6]);
  w2_issue_range<30, W2_AG>(c, w2);
#pragma unroll
  for (int t = 0; t < 6; ++t) pin(bv[t]);
#pragma unroll
  for (int t = 0; t < 5; ++t) pin(b2v[t]);
  group_wait(gfa, wave, lane);
  const int mp = 16 * (4 * i5 + wl) + (lane & 15);
  const int s = (mp >> 3) + 256 * (mp & 7);
  {
    u32x4 a[8][5];
    g1_prologue2<0>(c, a);
    f32x4 acc[2][6];
#pragma unroll
    for (int t = 0; t < 6; ++t) { acc[0][t] = f32x4{0, 0, 0, 0}; acc[1][t] = f32x4{0, 0, 0, 0}; }
    u32x4* hdst = Hf + ((size_t)(8 * i + 2 * wl1) * 48 + 3 * j) * 64 + lane;
    G1Epi e{bv, hdst, u32x4{0, 0, 0, 0}};
    g1_half<0>(c, a, w1, w2, e, acc); g1_half<1>(c, a, w1, w2, e, acc); g1_half<2>(c, a, w1, w2, e, acc); g1_half<3>(c, a, w1, w2, e, acc);
    g1_half<4>(c, a, w1, w2, e, acc); g1_half<5>(c, a, w1, w2, e, acc); g1_half<6>(c, a, w1, w2, e, acc);
    g1_half<7>(c, a, w1, w2, e, acc);
    e.dst = hdst + 48 * 64;
    g1_epi_from<0>(acc[1], e);
  }
  group_publish(gfb, j, tid);
  w2_issue_range<W2_AG, 60>(c, w2);
  group_wait(gfb, wave, lane);
  f32x4 acc[2][3];
#pragma unroll
  for (int t = 0; t < 3; ++t) { acc[0][t] = f32x4{0, 0, 0, 0}; acc[1][t] = f32x4{0, 0, 0, 0}; }
  const G2Epi e2{b2v, hv, out + s * 640 + 80 * jd + 4 * h, (const uint16_t*)hbuf + s * 640 + 80 * jd + 4 * h};
  {
    u32x4 hf[3][12];
    h_prologue<0>(c, hf);
    g2_half<0>(c, hf, w2, acc, e2); g2_half<1>(c, hf, w2, acc, e2); g2_half<2>(c, hf, w2, acc, e2); g2_half<3>(c, hf, w2, acc, e2);
    g2_half<4>(c, hf, w2, acc, e2); g2_half<5>(c, hf, w2, acc, e2); g2_half<6>(c, hf, w2, acc, e2);
    VMWAIT(0);
#pragma unroll
    for (int t = 0; t < 5; ++t) pin(hv[t]);
    g2_half<7>(c, hf, w2, acc, e2);
  }
  g2_epi_unit<3>(acc[1][0], e2);
  g2_epi_unit<4>(acc[1][1], e2);
}

extern "C" void kernel_launch(void* const* d_in, const int* in_sizes, int n_in, void* d_out, int out_size, void* d_ws,
                              size_t ws_size, hipStream_t stream) {
  const uint32_t* x = (const uint32_t*)d_in[0];
  const uint16_t* wr = (const uint16_t*)d_in[1];
  const uint16_t* wi = (const uint16_t*)d_in[2];
  const uint16_t* w1 = (const uint16_t*)d_in[4];
  const uint16_t* b1 = (const uint16_t*)d_in[5];
  const uint16_t* w2 = (const uint16_t*)d_in[6];
  const uint16_t* b2 = (const uint16_t*)d_in[7];
  char* ws = (char*)d_ws;
  size_t off = 0;
  auto carve = [&](size_t n) { char* p = ws + off; off += (n + 4095) & ~(size_t)4095; return p; };
  f2* T = (f2*)carve(5242880);
  f2* T2 = (f2*)carve(5242880);
  u32x4* Wt = (u32x4*)carve(2621440);
  u32x4* Wx = (u32x4*)carve(32768 + 4096);
  u32x4* W1f = (u32x4*)carve(1966080);
  u32x4* W2f = (u32x4*)carve(1966080);
  u32x4* L2f = (u32x4*)carve(2621440);
  uint32_t* hbuf = (uint32_t*)carve(2621440);
  u32x4* Hf = (u32x4*)carve(6291456);
  unsigned* flags = (unsigned*)carve(131072);
    k1_kernel<<<256, 512, 0, stream>>>(x, wr, wi, T, Wt, Wx);
  k2_kernel<<<256, 512, 0, stream>>>(T, T2, Wt, Wx, w1, w2, W1f, W2f, flags);
  k345_kernel<<<256, 256, 0, stream>>>(x, T2, hbuf, L2f, W1f, b1, Hf, W2f, b2, (uint16_t*)d_out, flags);
}
```
